```python
import torch

def _softmax(x, axis=-1):
    return torch.softmax(x, dim=axis)

def _gelu(x):
    return torch.nn.functional.gelu(x, approximate='tanh')

def _silu(x):
    return torch.nn.functional.silu(x)

def _sigmoid(x):
    return torch.sigmoid(x)

def _layernorm(x, axis=-1, eps=1e-05):
    m = x.mean(dim=axis, keepdim=True)
    v = x.var(dim=axis, keepdim=True, unbiased=False)
    return (x - m) / torch.sqrt(v + eps)

def _scatter_add(tgt, idx, src, axis=0):
    return tgt.clone().index_add_(axis, idx, src)

def _segment_sum(src, seg, n):
    out = torch.zeros((n,) + src.shape[1:], dtype=src.dtype)
    return out.index_add_(0, seg, src)

def _pad(x, pw):
    flat = tuple((v for pair in reversed(pw) for v in pair))
    return torch.nn.functional.pad(x, flat)
kernel_name = 'parallel_sum__memory_aug_recurrent_mem_tr__glu_gated_geglu__prompt'

def setup_inputs():

    _w = lambda a: (lambda t: t.to(torch.bfloat16) if t.is_floating_point() else t)(torch.as_tensor(a))

    def _A_setup():
        B, S, Dm = (1, 2048, 576)
        H, Dh = (32, 128)
        M, seg = (8, 32)
        seg = min(seg, S)
        S = S // seg * seg
        return {'x': _w((lambda *s: torch.randn(*s, dtype=torch.bfloat16) * 0.05)(B, S, Dm)), 'mem0': _w((lambda *s: torch.randn(*s, dtype=torch.bfloat16) * 0.2)(B, M, Dm)), 'Wq': _w((lambda *s: torch.randn(*s, dtype=torch.bfloat16) * 1.6)(Dm, H, Dh) * Dm ** (-0.5)), 'Wk': _w((lambda *s: torch.randn(*s, dtype=torch.bfloat16) * 1.6)(Dm, H, Dh) * Dm ** (-0.5)), 'Wv': _w((lambda *s: torch.randn(*s, dtype=torch.bfloat16) * 0.2)(Dm, H, Dh) * Dm ** (-0.5)), 'Wo': _w((lambda *s: torch.randn(*s, dtype=torch.bfloat16) * 0.2)(H, Dh, Dm) * (H * Dh) ** (-0.5)), 'seg_len': seg}
    _A_in = _A_setup()

    def _B_setup():
        B, S, Dm, Df = (1, 2048, 576, 4608)
        return {'x': _w((lambda *s: torch.randn(*s, dtype=torch.bfloat16) * 0.05)(B, S, Dm)), 'w_gate': _w((lambda *s: torch.randn(*s, dtype=torch.bfloat16) * 0.4)(Dm, Df) * Dm ** (-0.5)), 'w_up': _w((lambda *s: torch.randn(*s, dtype=torch.bfloat16) * 0.4)(Dm, Df) * Dm ** (-0.5)), 'w_down': _w((lambda *s: torch.randn(*s, dtype=torch.bfloat16) * 0.4)(Df, Dm) * Df ** (-0.5))}
    _B_in = _B_setup()
    return {'A_x': _A_in['x'], 'A_mem0': _A_in['mem0'], 'A_Wq': _A_in['Wq'], 'A_Wk': _A_in['Wk'], 'A_Wv': _A_in['Wv'], 'A_Wo': _A_in['Wo'], 'A_seg_len': _A_in['seg_len'], 'B_x': _B_in['x'], 'B_w_gate': _B_in['w_gate'], 'B_w_up': _B_in['w_up'], 'B_w_down': _B_in['w_down']}

def reference(A_x, A_mem0, A_Wq, A_Wk, A_Wv, A_Wo, A_seg_len, B_x, B_w_gate, B_w_up, B_w_down):

    def _A_ref(x, mem0, Wq, Wk, Wv, Wo, seg_len):
        B, S, Dm = x.shape
        H, Dh = (Wq.shape[1], Wq.shape[2])
        M = mem0.shape[1]
        scale = Dh ** (-0.5)
        n_seg = S // seg_len
        L = M + seg_len
        idx = torch.arange(L)
        mask = (idx[:, None] < M) | (idx[:, None] >= idx[None, :])
        mem = mem0
        outs = []
        for s in range(n_seg):
            xs = x[:, s * seg_len:(s + 1) * seg_len, :].clone()
            h = torch.concatenate([mem, xs], axis=1)
            q = torch.einsum('btd,dhe->bhte', h, Wq)
            k = torch.einsum('btd,dhe->bhte', h, Wk)
            v = torch.einsum('btd,dhe->bhte', h, Wv)
            scores = torch.einsum('bhqe,bhke->bhqk', q, k) * scale
            scores = torch.where(mask[None, None], scores, -1000000000.0)
            p = _softmax(scores, axis=-1)
            o = torch.einsum('bhqk,bhke->bhqe', p, v)
            o = torch.einsum('bhte,hed->btd', o, Wo)
            mem = o[:, :M, :].clone()
            outs.append(o[:, M:, :])
        return torch.concatenate(outs, axis=1)

    def _B_ref(x, w_gate, w_up, w_down):
        g = torch.einsum('bsd,df->bsf', x, w_gate)
        u = torch.einsum('bsd,df->bsf', x, w_up)
        h = _gelu(g) * u
        return torch.einsum('bsf,fd->bsd', h, w_down)
    x = A_x
    xn = _layernorm(x, axis=-1)
    return x + _A_ref(xn, A_mem0, A_Wq, A_Wk, A_Wv, A_Wo, A_seg_len) + _B_ref(xn, B_w_gate, B_w_up, B_w_down)
```

```cpp
#include <hip/hip_runtime.h>
#include <hip/hip_bf16.h>
typedef unsigned short u16;
__device__ __forceinline__ float bf2f(u16 h) { return __uint_as_float((unsigned)h << 16); }
__device__ __forceinline__ u16 f2bf(float f) { unsigned u = __float_as_uint(f); u += 0x7FFFu + ((u >> 16) & 1u); return (u16)(u >> 16); }
constexpr int S = 2048, D = 576, H = 32, DH = 128, HD = 4096, DF = 4608, M = 8, SEG = 32, NSEG = 64;

__global__ void ln_k(const u16* x, u16* xn) {
  int r = blockIdx.x, t = threadIdx.x; __shared__ float red[64];
  float v[9]; float s = 0; for (int i = 0; i < 9; ++i) { v[i] = bf2f(x[r * D + t + 64 * i]); s += v[i]; }
  for (int o = 32; o; o >>= 1) s += __shfl_xor(s, o);
  float m = s / D; float q = 0; for (int i = 0; i < 9; ++i) { float d = v[i] - m; q += d * d; }
  for (int o = 32; o; o >>= 1) q += __shfl_xor(q, o);
  float rs = rsqrtf(q / D + 1e-5f);
  for (int i = 0; i < 9; ++i) xn[r * D + t + 64 * i] = f2bf((v[i] - m) * rs);
}
template <int MODE>
__global__ void __launch_bounds__(256) gemm_k(const u16* A, int lda, const u16* B, const u16* B2, int ldb, u16* C, int ldc, int K, const u16* add1, const u16* add2) {
  __shared__ float As[16][65], Bs[16][64], Bs2[16][64];
  int tx = threadIdx.x & 15, ty = threadIdx.x >> 4, m0 = blockIdx.y * 64, n0 = blockIdx.x * 64;
  float acc[4][4] = {}, acc2[4][4] = {};
  for (int k0 = 0; k0 < K; k0 += 16) {
    for (int i = threadIdx.x; i < 1024; i += 256) { int r = i >> 4, c = i & 15; As[c][r] = bf2f(A[(size_t)(m0 + r) * lda + k0 + c]); }
    for (int i = threadIdx.x; i < 1024; i += 256) { int r = i >> 6, c = i & 63; Bs[r][c] = bf2f(B[(size_t)(k0 + r) * ldb + n0 + c]); if (MODE == 1) Bs2[r][c] = bf2f(B2[(size_t)(k0 + r) * ldb + n0 + c]); }
    __syncthreads();
    for (int k = 0; k < 16; ++k) for (int i = 0; i < 4; ++i) for (int j = 0; j < 4; ++j) { acc[i][j] += As[k][ty * 4 + i] * Bs[k][tx * 4 + j]; if (MODE == 1) acc2[i][j] += As[k][ty * 4 + i] * Bs2[k][tx * 4 + j]; }
    __syncthreads();
  }
  for (int i = 0; i < 4; ++i) for (int j = 0; j < 4; ++j) {
    size_t o = (size_t)(m0 + ty * 4 + i) * ldc + n0 + tx * 4 + j; float v = acc[i][j];
    if (MODE == 1) { float g = v; float t = tanhf(0.7978845608f * (g + 0.044715f * g * g * g)); v = 0.5f * g * (1.f + t) * acc2[i][j]; }
    if (MODE == 2) v += bf2f(add1[o]) + bf2f(add2[o]);
    C[o] = f2bf(v);
  }
}
__global__ void __launch_bounds__(128) step_k(const u16* mem, const u16* Wq, const u16* Wk, const u16* Wv, const u16* Q, const u16* Kx, const u16* Vx, u16* Ox, u16* Om, int s) {
  int h = blockIdx.x, t = threadIdx.x;
  __shared__ float q[40][128], k[40][129], v[40][128], p[40][41], ms[8][576];
  for (int i = t; i < 8 * 576; i += 128) ms[i / 576][i % 576] = bf2f(mem[i]);
  __syncthreads();
  float aq[8] = {}, ak[8] = {}, av[8] = {};
  for (int d = 0; d < D; ++d) { float wq = bf2f(Wq[d * HD + h * DH + t]), wk = bf2f(Wk[d * HD + h * DH + t]), wv = bf2f(Wv[d * HD + h * DH + t]);
    for (int r = 0; r < 8; ++r) { aq[r] += ms[r][d] * wq; ak[r] += ms[r][d] * wk; av[r] += ms[r][d] * wv; } }
  for (int r = 0; r < 8; ++r) { q[r][t] = bf2f(f2bf(aq[r])); k[r][t] = bf2f(f2bf(ak[r])); v[r][t] = bf2f(f2bf(av[r])); }
  for (int r = 0; r < 32; ++r) { size_t o = (size_t)(s * 32 + r) * HD + h * DH + t; q[8 + r][t] = bf2f(Q[o]); k[8 + r][t] = bf2f(Kx[o]); v[8 + r][t] = bf2f(Vx[o]); }
  __syncthreads();
  for (int i = t; i < 1600; i += 128) { int a = i / 40, b = i % 40; float sc = 0; for (int e = 0; e < 128; ++e) sc += q[a][e] * k[b][e]; p[a][b] = (a < 8 || a >= b) ? sc * 0.08838834764831845f : -1e9f; }
  __syncthreads();
  if (t < 40) { float mx = -3e38f; for (int b = 0; b < 40; ++b) mx = fmaxf(mx, p[t][b]); float sm = 0; for (int b = 0; b < 40; ++b) { float e = __expf(p[t][b] - mx); p[t][b] = e; sm += e; } for (int b = 0; b < 40; ++b) p[t][b] /= sm; }
  __syncthreads();
  for (int a = 0; a < 40; ++a) { float o = 0; for (int b = 0; b < 40; ++b) o += p[a][b] * v[b][t]; if (a < 8) Om[a * HD + h * DH + t] = f2bf(o); else Ox[(size_t)(s * 32 + a - 8) * HD + h * DH + t] = f2bf(o); }
}
__global__ void __launch_bounds__(256) memo_k(const u16* Om, const u16* Wo, u16* mem) {
  int c = blockIdx.x, t = threadIdx.x;
  __shared__ float red[8][256];
  float a[8] = {};
  for (int e = t; e < HD; e += 256) { float w = bf2f(Wo[(size_t)e * D + c]); for (int r = 0; r < 8; ++r) a[r] += bf2f(Om[r * HD + e]) * w; }
  for (int r = 0; r < 8; ++r) red[r][t] = a[r];
  __syncthreads();
  if (t < 8) { float s = 0; for (int i = 0; i < 256; ++i) s += red[t][i]; mem[t * D + c] = f2bf(s); }
}
extern "C" void kernel_launch(void* const* d_in, const int* in_sizes, int n_in, void* d_out, int out_size, void* d_ws, size_t ws_size, hipStream_t stream) {
  const u16* x = (const u16*)d_in[0]; const u16* mem0 = (const u16*)d_in[1];
  const u16 *Wq = (const u16*)d_in[2], *Wk = (const u16*)d_in[3], *Wv = (const u16*)d_in[4], *Wo = (const u16*)d_in[5];
  const u16 *Wg = (const u16*)d_in[8], *Wu = (const u16*)d_in[9], *Wd = (const u16*)d_in[10];
  u16* ws = (u16*)d_ws;
  u16* xn = ws; ws += S * D; u16* Q = ws; ws += (size_t)S * HD; u16* K = ws; ws += (size_t)S * HD; u16* V = ws; ws += (size_t)S * HD;
  u16* Hh = ws; ws += (size_t)S * DF; u16* Ox = ws; ws += (size_t)S * HD; u16* Om = ws; ws += 8 * HD; u16* mem = ws; ws += 8 * D; u16* F = ws; ws += S * D; u16* Z = ws; ws += S * D;
  ln_k<<<S, 64, 0, stream>>>(x, xn);
  gemm_k<0><<<dim3(HD / 64, S / 64), 256, 0, stream>>>(xn, D, Wq, nullptr, HD, Q, HD, D, nullptr, nullptr);
  gemm_k<0><<<dim3(HD / 64, S / 64), 256, 0, stream>>>(xn, D, Wk, nullptr, HD, K, HD, D, nullptr, nullptr);
  gemm_k<0><<<dim3(HD / 64, S / 64), 256, 0, stream>>>(xn, D, Wv, nullptr, HD, V, HD, D, nullptr, nullptr);
  gemm_k<1><<<dim3(DF / 64, S / 64), 256, 0, stream>>>(xn, D, Wg, Wu, DF, Hh, DF, D, nullptr, nullptr);
  gemm_k<0><<<dim3(D / 64, S / 64), 256, 0, stream>>>(Hh, DF, Wd, nullptr, D, F, D, DF, nullptr, nullptr);
  for (int s = 0; s < NSEG; ++s) {
    step_k<<<H, 128, 0, stream>>>(s == 0 ? mem0 : mem, Wq, Wk, Wv, Q, K, V, Ox, Om, s);
    if (s + 1 < NSEG) memo_k<<<D, 256, 0, stream>>>(Om, Wo, mem);
  }
  gemm_k<2><<<dim3(D / 64, S / 64), 256, 0, stream>>>(Ox, HD, Wo, nullptr, D, (u16*)d_out, D, HD, x, F);
}
```

```cpp
#include <hip/hip_runtime.h>
#include <hip/hip_bf16.h>
typedef unsigned short u16;
typedef unsigned v4u __attribute__((ext_vector_type(4)));
typedef unsigned v2u __attribute__((ext_vector_type(2)));
typedef unsigned v8u __attribute__((ext_vector_type(8)));
typedef float f32x4 __attribute__((ext_vector_type(4)));
typedef float f32x2 __attribute__((ext_vector_type(2)));
typedef __bf16 bf16x2 __attribute__((ext_vector_type(2)));
typedef __bf16 bf16x8 __attribute__((ext_vector_type(8)));
typedef __bf16 bf16x16 __attribute__((ext_vector_type(16)));
typedef short s16x4 __attribute__((ext_vector_type(4)));
#define LAS __attribute__((address_space(3)))
#define GAS __attribute__((address_space(1)))
typedef unsigned lptr;
#define LP(T, p) ((LAS T*)(unsigned long)(p))
typedef GAS unsigned gu32;
#define RLX_AGENT __ATOMIC_RELAXED, __HIP_MEMORY_SCOPE_AGENT
#define DEVI __device__ __forceinline__
#define BAR() do { __builtin_amdgcn_sched_barrier(0); asm volatile("s_waitcnt lgkmcnt(0)" ::: "memory"); __builtin_amdgcn_s_barrier(); asm volatile("" ::: "memory"); __builtin_amdgcn_sched_barrier(0); } while (0)
#define BARN() do { asm volatile("" ::: "memory"); __builtin_amdgcn_sched_barrier(0); __builtin_amdgcn_s_barrier(); __builtin_amdgcn_sched_barrier(0); asm volatile("" ::: "memory"); } while (0)
#define SCHED0() __builtin_amdgcn_sched_barrier(0)
#define PRIO(x) __builtin_amdgcn_s_setprio(x)
#define OPAQUE(x) asm volatile("" : "+v"(x))

#define NWG 256
constexpr int S_ = 2048, D_ = 576, HD_ = 4096, DF_ = 4608;
constexpr unsigned WIN = 32768u;
constexpr unsigned WS_PART = 2u << 20, PART_SLOT = 2u * WIN;
constexpr unsigned WS_MEMB = WS_PART + 36u * WIN, MEM_SLOT = WIN;
constexpr unsigned WS_TIME = WS_MEMB + 3072u;
constexpr unsigned WS_FLG = WS_PART + 48u * WIN;
constexpr unsigned FS = 256u;
constexpr unsigned F_LN = WS_FLG, F_QKV = F_LN + 64u * FS, F_H = F_QKV + 64u * FS, F_O = F_H + 64u * FS, F_E = F_O + 64u * FS, F_Q0 = F_E + 128u * FS;
constexpr unsigned H_CH = WS_FLG + 512u * FS, H_R = H_CH + 4096u, H_G = H_R + 4096u;
constexpr unsigned WS_EP = H_G + 4096u, EP_SLOT = 9u * 64u * 1024u;
constexpr unsigned WS_ARM_END = WS_EP + 4u * EP_SLOT;
constexpr unsigned SEGB = 262144u, HB = 8192u, MATB = 16u << 20, XSEG = 36864u, HSEG = 294912u;
constexpr unsigned WS_XN = 92u << 20;
static_assert(WS_ARM_END <= (8u << 20), "workspace map");
constexpr unsigned WS_Q = 8u << 20, WS_K = WS_Q + MATB, WS_V = WS_K + MATB, WS_O = WS_V + MATB;
constexpr unsigned WS_H = 72u << 20;
constexpr unsigned WS_DIAG = 128u << 20;
constexpr unsigned WS_TOTAL = 254u << 20;

struct Params {
  const u16 *x, *mem0, *Wq, *Wk, *Wv, *Wo, *Wg, *Wu, *Wd;
  u16* out; unsigned char* ws;
  int t0, t1, t2, t3;
};

#define NTM 0
template <int M> DEVI v4u gl128(const u16* a) { if (NTM & M) return __builtin_nontemporal_load((const GAS v4u*)a); else return *(const GAS v4u*)a; }
#define AUX(M) ((NTM & (M)) ? 2 : 0)
DEVI void dma16(const void* a, unsigned lds) { asm volatile("s_mov_b32 m0, %1\n\ts_nop 0\n\tglobal_load_lds_dwordx4 %0, off" :: "v"(a), "s"(lds) : "memory", "m0"); }
DEVI void gla(v4u& d, const void* a) { asm volatile("global_load_dwordx4 %0, %1, off" : "=v"(d) : "v"(a) : "memory"); }
DEVI unsigned pk(float a, float b) { f32x2 x = {a, b}; return __builtin_bit_cast(unsigned, __builtin_convertvector(x, bf16x2)); }
DEVI unsigned fu(float f) { return __float_as_uint(f); }
DEVI float uf(unsigned u) { return __uint_as_float(u); }
DEVI v4u ldsr128(lptr p) { return *LP(const v4u, p); }
DEVI void ldsw128(lptr p, v4u v) { *LP(v4u, p) = v; }
DEVI v2u ldsr64(lptr p) { return *LP(const v2u, p); }
DEVI void ldsw64(lptr p, v2u v) { *LP(v2u, p) = v; }
DEVI void ldsw32(lptr p, unsigned v) { *LP(unsigned, p) = v; }
DEVI v2u ldstr(lptr p) { return __builtin_bit_cast(v2u, __builtin_amdgcn_ds_read_tr16_b64_v4i16(LP(s16x4, p))); }
DEVI v8u cat44(v4u a, v4u b) { v8u r = {a.x, a.y, a.z, a.w, b.x, b.y, b.z, b.w}; return r; }
DEVI v4u lo4(v8u a) { v4u r = {a[0], a[1], a[2], a[3]}; return r; }
DEVI v4u hi4(v8u a) { v4u r = {a[4], a[5], a[6], a[7]}; return r; }
DEVI v4u cat4(v2u a, v2u b) { v4u r = {a.x, a.y, b.x, b.y}; return r; }
DEVI v8u cat8(v2u a, v2u b, v2u c, v2u d) { v8u r = {a.x, a.y, b.x, b.y, c.x, c.y, d.x, d.y}; return r; }
DEVI f32x4 smf(v4u a, v8u b, f32x4 c, int idx) { return __builtin_amdgcn_smfmac_f32_16x16x64_bf16(__builtin_bit_cast(bf16x8, a), __builtin_bit_cast(bf16x16, b), c, idx, 0, 0); }
DEVI f32x4 mf32(v4u a, v4u b, f32x4 c) { return __builtin_amdgcn_mfma_f32_16x16x32_bf16(__builtin_bit_cast(bf16x8, a), __builtin_bit_cast(bf16x8, b), c, 0, 0, 0); }
DEVI f32x4 mf16(v2u a, v2u b, f32x4 c) { return __builtin_amdgcn_mfma_f32_16x16x16bf16_1k(__builtin_bit_cast(s16x4, a), __builtin_bit_cast(s16x4, b), c, 0, 0, 0); }
DEVI float fold32(float a, float b) { auto r = __builtin_amdgcn_permlane32_swap(fu(a), fu(b), false, false); return uf(r[0]) + uf(r[1]); }
DEVI float mx2(float a, float b) { return __builtin_elementwise_maximum(a, b); }
DEVI float mx3(float a, float b, float c) { return mx2(mx2(a, b), c); }
DEVI float max16(float a) { auto r = __builtin_amdgcn_permlane16_swap(fu(a), fu(a), false, false); return mx2(uf(r[0]), uf(r[1])); }
DEVI float max32(float a) { auto r = __builtin_amdgcn_permlane32_swap(fu(a), fu(a), false, false); return mx2(uf(r[0]), uf(r[1])); }
DEVI float ror8(float a) { return uf(__builtin_amdgcn_update_dpp(0, fu(a), 0x128, 0xf, 0xf, false)); }
DEVI unsigned rt() { return (unsigned)__builtin_amdgcn_s_memrealtime(); }
DEVI unsigned clk() { return (unsigned)__builtin_amdgcn_s_memtime(); }
DEVI unsigned xcc_id() { return (unsigned)__builtin_amdgcn_s_getreg(20 | (3 << 11)) & 0xfu; }
DEVI unsigned max4(v4u a) { return max(max(a.x, a.y), max(a.z, a.w)); }
DEVI v4u canon(v4u a) { v4u r = {min(a.x, 0xFFFFFFFEu), min(a.y, 0xFFFFFFFEu), min(a.z, 0xFFFFFFFEu), min(a.w, 0xFFFFFFFEu)}; return r; }
typedef __amdgpu_buffer_rsrc_t rsrc_t;
DEVI v4u bl128(rsrc_t r, unsigned vo, unsigned so) { return __builtin_amdgcn_raw_buffer_load_b128(r, vo, so, 16); }
DEVI void bs128(v4u v, rsrc_t r, unsigned vo, unsigned so) { __builtin_amdgcn_raw_buffer_store_b128(v, r, vo, so, 16); }
#define LANE_ID(tag) ({ int l_; asm volatile("v_mbcnt_lo_u32_b32 %0, -1, 0\n\tv_mbcnt_hi_u32_b32 %0, -1, %0 ; " tag : "=v"(l_)); l_; })
constexpr unsigned SPINCAP = 0x2000;
constexpr float SM_C = 0.08838834764831845f * 1.4426950408889634f;

constexpr unsigned XB = 8192, VB = 4096;
constexpr unsigned L_MEM = 0, L_OC = 9216, L_KX = 10240, L_QX = L_KX + 2 * XB, L_VX = L_QX + 2 * XB, L_QI = L_VX + 2 * VB, L_KI = L_QI + 2048, L_VM = L_KI + 4096, L_OI = L_VM + 2048, L_SEL = L_OI + 2048, L_FL = L_SEL + 1024, L_WV = L_FL + 1024, L_WO = L_WV + 73728, L_HS = L_WO + 24576, L_END = L_HS + 64;

#define T2(x) 0
#define LST(k)

DEVI v4u sel_matrix(int n) { const unsigned one = 0x3f80u << ((n & 1) * 16), m = (n & 7) >> 1; v4u sel = {m == 0 ? one : 0, m == 1 ? one : 0, m == 2 ? one : 0, m == 3 ? one : 0}; return sel; }
DEVI void bs32(unsigned v, rsrc_t r, unsigned vo, unsigned so) { __builtin_amdgcn_raw_buffer_store_b32(v, r, vo, so, 16); }
#define LANE_VARS const int g = lane >> 4, n = lane & 15, q4 = (lane >> 2) & 3, p4 = lane & 3; (void)g; (void)n; (void)q4; (void)p4; const unsigned l16 = lane * 16
DEVI unsigned part_base(unsigned r) { const unsigned i = r >> 3; return WS_PART + i * 4u * WIN + (((r & 7) ^ (i >> 3) ^ (i & 4)) << 12); }
#define R_OFF(r) (WS_MEMB + ((r) / 24u) * 4u * WIN + (2 * (lane >> 5) + ((lane >> 3) & 1)) * 4096u + ((r) % 24u) * 128u + (4 * ((lane >> 4) & 1) + (lane & 3)) * 16u + ((lane >> 2) & 1) * 8u)
#define ST_OFF (part_base(8 * w + 4 * (g >> 1) + (n >> 2)) + (unsigned)((b >> 5) * WIN + (b & 31) * 128 + (4 * (g & 1) + (n & 3)) * 16))
#define ST5_OFF (part_base(64 + 4 * (w3 & 1) + 2 * (g >> 1) + (n >> 3)) + (unsigned)((b >> 5) * WIN + (b & 31) * 128 + (4 * (g & 1) + (n & 3)) * 16 + ((n >> 2) & 1) * 8))
#define G_MAIN(j) (WS_MEMB + (w / 3) * 4 * WIN + (j) * 4096 + (w % 3) * 1024)
#define G_EXTRA(j) (WS_MEMB + 8 * WIN + (j) * 4096 + 2048 + w * 128)
#define WAITCLK(t, d) do { while (clk() - (t) < (unsigned)(d)) { } } while (0)
typedef unsigned v16u __attribute__((ext_vector_type(16)));
DEVI v16u cat88(v8u a, v8u b) { return __builtin_shufflevector(a, b, 0, 1, 2, 3, 4, 5, 6, 7, 8, 9, 10, 11, 12, 13, 14, 15); }
#define CHAIN_Q_ASM \
  ".Lfkq0:\n" \
  "  s_mov_b32 s40, 0\n" \
  "  s_mov_b32 s41, 0\n" \
  "  s_mov_b32 s42, 0\n" \
  "  s_mov_b32 s43, 8192\n" \
  "  s_mov_b32 s44, 4096\n" \
  "  s_mov_b32 s66, 1040356334\n" \
  "  s_mov_b32 s67, 4\n" \
  "  s_mov_b32 s68, 0x100000\n" \
  "  s_mov_b32 s29, 0\n" \
  "  s_mov_b32 s26, 0\n" \
  "  v_cmp_ge_i32_e64 s[50:51], v192, 0\n" \
  "  v_cmp_ge_i32_e64 s[58:59], v193, 0\n" \
  "  v_cmp_ge_i32_e64 s[52:53], v192, 1\n" \
  "  v_cmp_ge_i32_e64 s[60:61], v193, 1\n" \
  "  v_cmp_ge_i32_e64 s[54:55], v192, 2\n" \
  "  v_cmp_ge_i32_e64 s[62:63], v193, 2\n" \
  "  v_cmp_ge_i32_e64 s[56:57], v192, 3\n" \
  "  v_cmp_ge_i32_e64 s[64:65], v193, 3\n" \
  "  v_mov_b64_e32 v[228:229], 0\n" \
  "  v_mov_b64_e32 v[230:231], 0\n" \
  "  v_mov_b64_e32 v[232:233], 0\n" \
  "  v_mov_b64_e32 v[234:235], 0\n" \
  "  s_setprio 3\n" \
  "  s_barrier\n" \
  "  ds_read_b128 v[236:239], v180 offset:10240\n" \
  "  ds_read_b128 v[240:243], v180 offset:10496\n" \
  "  ds_read_b128 v[244:247], v180 offset:12288\n" \
  "  ds_read_b128 v[248:251], v180 offset:12544\n" \
  "  ds_read_b128 v[252:255], v180 offset:14336\n" \
  "  s_branch LQb_%=\n" \
  "LQ_%=:\n" \
  "  s_setprio 3\n" \
  "  s_waitcnt lgkmcnt(0)\n" \
  "  s_barrier\n" \
  "  buffer_load_dwordx4 v[192:195], v176, s[36:39], s23 offen sc1\n" \
  "  buffer_load_dword v196, v197, s[36:39], s24 offen sc1\n" \
  "  ds_read_b128 v[236:239], v180 offset:10240\n" \
  "  ds_read_b128 v[240:243], v180 offset:10496\n" \
  "  ds_read_b128 v[244:247], v180 offset:12288\n" \
  "  ds_read_b128 v[248:251], v180 offset:12544\n" \
  "  ds_read_b128 v[252:255], v180 offset:14336\n" \
  "  s_waitcnt vmcnt(1)\n" \
  "  ds_write_b128 v198, v[192:195] offset:0\n" \
  "  v_max3_u32 v200, v192, v193, v194\n" \
  "  s_waitcnt vmcnt(0)\n" \
  "  ds_write_b32 v199, v196 offset:8192\n" \
  "  v_max3_u32 v200, v200, v195, v196\n" \
  "  v_cmp_eq_u32_e32 vcc, -1, v200\n" \
  "  s_cbranch_vccnz Lhq_%=\n" \
  "LQb_%=:\n" \
  "  s_waitcnt lgkmcnt(0)\n" \
  "  s_barrier\n" \
  "  ds_read_b128 v[192:195], v176 offset:0\n" \
  "  ds_read_b128 v[196:199], v176 offset:1024\n" \
  "  ds_read_b128 v[200:203], v176 offset:2048\n" \
  "  ds_read_b128 v[204:207], v176 offset:3072\n" \
  "  buffer_store_dwordx4 v[224:227], v184, s[36:39], s42 offen sc1\n" \
  "  s_waitcnt lgkmcnt(3)\n" \
  "  v_smfmac_f32_16x16x64_bf16 v[228:231], v[192:195], v[0:7], v177\n" \
  "  v_smfmac_f32_16x16x64_bf16 v[232:235], v[192:195], v[8:15], v177\n" \
  "  ds_read_b128 v[208:211], v176 offset:4096\n" \
  "  s_waitcnt lgkmcnt(3)\n" \
  "  v_smfmac_f32_16x16x64_bf16 v[228:231], v[196:199], v[16:23], v177\n" \
  "  v_smfmac_f32_16x16x64_bf16 v[232:235], v[196:199], v[24:31], v177\n" \
  "  ds_read_b128 v[212:215], v176 offset:5120\n" \
  "  s_waitcnt lgkmcnt(3)\n" \
  "  v_smfmac_f32_16x16x64_bf16 v[228:231], v[200:203], v[32:39], v177\n" \
  "  v_smfmac_f32_16x16x64_bf16 v[232:235], v[200:203], v[40:47], v177\n" \
  "  ds_read_b128 v[216:219], v176 offset:6144\n" \
  "  s_waitcnt lgkmcnt(3)\n" \
  "  v_smfmac_f32_16x16x64_bf16 v[228:231], v[204:207], v[48:55], v177\n" \
  "  v_smfmac_f32_16x16x64_bf16 v[232:235], v[204:207], v[56:63], v177\n" \
  "  ds_read_b128 v[220:223], v176 offset:7168\n" \
  "  s_waitcnt lgkmcnt(3)\n" \
  "  v_smfmac_f32_16x16x64_bf16 v[228:231], v[208:211], v[64:71], v177\n" \
  "  ds_read_b128 v[224:227], v176 offset:8192\n" \
  "  v_smfmac_f32_16x16x64_bf16 v[232:235], v[208:211], v[72:79], v177\n" \
  "  ds_read_b128 v[192:195], v180 offset:14592\n" \
  "  s_waitcnt lgkmcnt(4)\n" \
  "  v_smfmac_f32_16x16x64_bf16 v[228:231], v[212:215], v[80:87], v177\n" \
  "  ds_read_b128 v[196:199], v180 offset:16384\n" \
  "  v_smfmac_f32_16x16x64_bf16 v[232:235], v[212:215], v[88:95], v177\n" \
  "  ds_read_b128 v[200:203], v180 offset:16640\n" \
  "  s_waitcnt lgkmcnt(5)\n" \
  "  v_smfmac_f32_16x16x64_bf16 v[228:231], v[216:219], v[96:103], v177\n" \
  "  v_smfmac_f32_16x16x64_bf16 v[232:235], v[216:219], v[104:111], v177\n" \
  "  s_waitcnt lgkmcnt(4)\n" \
  "  v_smfmac_f32_16x16x64_bf16 v[228:231], v[220:223], v[112:119], v177\n" \
  "  v_smfmac_f32_16x16x64_bf16 v[232:235], v[220:223], v[120:127], v177\n" \
  "  s_waitcnt lgkmcnt(3)\n" \
  "  v_smfmac_f32_16x16x64_bf16 v[228:231], v[224:227], v[128:135], v177\n" \
  "  v_smfmac_f32_16x16x64_bf16 v[232:235], v[224:227], v[136:143], v177\n" \
  "  s_setprio 3\n" \
  "  s_nop 5\n" \
  "  v_add_f32_e32 v228, v228, v230\n" \
  "  v_add_f32_e32 v229, v229, v231\n" \
  "  v_add_f32_e32 v232, v232, v234\n" \
  "  v_add_f32_e32 v233, v233, v235\n" \
  "  v_cvt_pk_bf16_f32 v230, v228, v229\n" \
  "  v_cvt_pk_bf16_f32 v231, v232, v233\n" \
  "  ds_write2_b32 v183, v230, v231 offset1:64\n" \
  "  ds_read_b64_tr_b16 v[220:221], v181 offset:0\n" \
  "  ds_read_b64_tr_b16 v[222:223], v181 offset:256\n" \
  "  s_waitcnt lgkmcnt(2)\n" \
  "  s_barrier\n" \
  "  ds_read_b64_tr_b16 v[204:205], v179 offset:51200\n" \
  "  ds_read_b64_tr_b16 v[206:207], v179 offset:51264\n" \
  "  ds_read_b64_tr_b16 v[208:209], v179 offset:51712\n" \
  "  ds_read_b64_tr_b16 v[210:211], v179 offset:51776\n" \
  "  ds_read_b64_tr_b16 v[212:213], v179 offset:52224\n" \
  "  ds_read_b64_tr_b16 v[214:215], v179 offset:52288\n" \
  "  ds_read_b64_tr_b16 v[216:217], v179 offset:52736\n" \
  "  ds_read_b64_tr_b16 v[218:219], v179 offset:52800\n" \
  "  s_waitcnt lgkmcnt(6)\n" \
  "  v_mfma_f32_16x16x32_bf16 v[224:227], v[236:239], v[204:207], 0\n" \
  "  v_mfma_f32_16x16x32_bf16 v[228:231], v[240:243], v[204:207], 0\n" \
  "  s_waitcnt lgkmcnt(4)\n" \
  "  v_mfma_f32_16x16x32_bf16 v[224:227], v[244:247], v[208:211], v[224:227]\n" \
  "  v_mfma_f32_16x16x32_bf16 v[228:231], v[248:251], v[208:211], v[228:231]\n" \
  "  s_waitcnt lgkmcnt(2)\n" \
  "  v_mfma_f32_16x16x32_bf16 v[224:227], v[252:255], v[212:215], v[224:227]\n" \
  "  v_mfma_f32_16x16x32_bf16 v[228:231], v[192:195], v[212:215], v[228:231]\n" \
  "  s_waitcnt lgkmcnt(0)\n" \
  "  v_mfma_f32_16x16x32_bf16 v[224:227], v[196:199], v[216:219], v[224:227]\n" \
  "  v_mfma_f32_16x16x32_bf16 v[228:231], v[200:203], v[216:219], v[228:231]\n" \
  "  s_barrier\n" \
  "  s_setprio 3\n" \
  "  ds_read_b64_tr_b16 v[236:237], v187 offset:53248\n" \
  "  ds_read_b64_tr_b16 v[238:239], v187 offset:53376\n" \
  "  ds_read_b64_tr_b16 v[240:241], v187 offset:54272\n" \
  "  ds_read_b64_tr_b16 v[242:243], v187 offset:54400\n" \
  "  ds_read_b64_tr_b16 v[244:245], v187 offset:55296\n" \
  "  ds_read_b64_tr_b16 v[246:247], v187 offset:55424\n" \
  "  ds_read_b64_tr_b16 v[248:249], v187 offset:56320\n" \
  "  ds_read_b64_tr_b16 v[250:251], v187 offset:56448\n" \
  "  v_max3_f32 v252, v224, v225, v226\n" \
  "  v_max3_f32 v253, v227, v228, v229\n" \
  "  v_max3_f32 v252, v252, v230, v231\n" \
  "  v_max_f32_e32 v254, v252, v253\n" \
  "  s_waitcnt lgkmcnt(6)\n" \
  "  v_mfma_f32_16x16x32_bf16 v[232:235], v[236:239], v[204:207], 0\n" \
  "  s_waitcnt lgkmcnt(4)\n" \
  "  v_mfma_f32_16x16x32_bf16 v[232:235], v[240:243], v[208:211], v[232:235]\n" \
  "  s_waitcnt lgkmcnt(2)\n" \
  "  v_mfma_f32_16x16x32_bf16 v[232:235], v[244:247], v[212:215], v[232:235]\n" \
  "  s_waitcnt lgkmcnt(0)\n" \
  "  v_mfma_f32_16x16x32_bf16 v[232:235], v[248:251], v[216:219], v[232:235]\n" \
  "  v_subrev_u32_e32 v214, s47, v185\n" \
  "  v_mov_b32_e32 v207, 0\n" \
  "  v_and_b32_e32 v215, 0x37f, v176\n" \
  "  s_nop 4\n" \
  "  v_max3_f32 v252, v254, v232, v233\n" \
  "  v_max3_f32 v253, v254, v232, v233\n" \
  "  s_nop 1\n" \
  "  v_permlane16_swap_b32_e32 v252, v253\n" \
  "  v_max_f32_e32 v254, v252, v253\n" \
  "  v_max_f32_e32 v253, v252, v253\n" \
  "  s_nop 1\n" \
  "  v_permlane32_swap_b32_e32 v254, v253\n" \
  "  v_max_f32_e32 v252, v254, v253\n" \
  "  v_mul_f32_e64 v254, v252, -s66\n" \
  "  v_fma_f32 v224, v224, s66, v254\n" \
  "  v_fma_f32 v225, v225, s66, v254\n" \
  "  v_exp_f32_e32 v224, v224\n" \
  "  v_fma_f32 v226, v226, s66, v254\n" \
  "  v_exp_f32_e32 v225, v225\n" \
  "  v_fma_f32 v227, v227, s66, v254\n" \
  "  ds_read_b32 v255, v176 offset:62472\n" \
  "  ds_read_b64 v[208:209], v185\n" \
  "  ds_read_b64 v[210:211], v214\n" \
  "  ds_read_b64 v[212:213], v214 offset:512\n" \
  "  v_exp_f32_e32 v226, v226\n" \
  "  v_fma_f32 v228, v228, s66, v254\n" \
  "  v_exp_f32_e32 v227, v227\n" \
  "  v_fma_f32 v229, v229, s66, v254\n" \
  "  v_cvt_pk_bf16_f32 v192, v224, v225\n" \
  "  v_exp_f32_e32 v228, v228\n" \
  "  v_fma_f32 v230, v230, s66, v254\n" \
  "  v_exp_f32_e32 v229, v229\n" \
  "  v_fma_f32 v231, v231, s66, v254\n" \
  "  v_cvt_pk_bf16_f32 v193, v226, v227\n" \
  "  v_exp_f32_e32 v230, v230\n" \
  "  v_fma_f32 v232, v232, s66, v254\n" \
  "  v_exp_f32_e32 v231, v231\n" \
  "  v_fma_f32 v233, v233, s66, v254\n" \
  "  v_cvt_pk_bf16_f32 v194, v228, v229\n" \
  "  v_exp_f32_e32 v232, v232\n" \
  "  v_cvt_pk_bf16_f32 v195, v230, v231\n" \
  "  v_exp_f32_e32 v233, v233\n" \
  "  s_nop 0\n" \
  "  v_mfma_f32_16x16x32_bf16 v[200:203], v[188:191], v[192:195], 0\n" \
  "  s_waitcnt lgkmcnt(3)\n" \
  "  v_cmp_gt_u32_e32 vcc, s67, v255\n" \
  "  v_cvt_pk_bf16_f32 v206, v232, v233\n" \
  "  v_mfma_f32_16x16x32_bf16 v[196:199], v[220:223], v[192:195], 0\n" \
  "  v_mov_b64_e32 v[224:225], 0\n" \
  "  v_mov_b64_e32 v[226:227], 0\n" \
  "  v_mfma_f32_16x16x16_bf16 v[200:203], v[188:189], v[206:207], v[200:203]\n" \
  "  s_cbranch_vccz LQ3c_%=\n" \
  "LQ3_%=:\n" \
  "  ds_read_b32 v255, v176 offset:62472\n" \
  "  ds_read_b64 v[208:209], v185\n" \
  "  ds_read_b64 v[210:211], v214\n" \
  "  ds_read_b64 v[212:213], v214 offset:512\n" \
  "  s_sub_u32 s68, s68, 1\n" \
  "  s_waitcnt lgkmcnt(3)\n" \
  "  v_cmp_gt_u32_e32 vcc, s67, v255\n" \
  "  s_cselect_b64 s[34:35], 0, -1\n" \
  "  s_cselect_b32 s68, 0, s68\n" \
  "  s_and_b64 vcc, vcc, s[34:35]\n" \
  "  s_cbranch_vccnz LQ3_%=\n" \
  "LQ3c_%=:\n" \
  "  s_waitcnt lgkmcnt(2)\n" \
  "  v_mfma_f32_16x16x16_bf16 v[196:199], v[208:209], v[206:207], v[196:199]\n" \
  "  v_mov_b64_e32 v[220:221], 0\n" \
  "  v_mov_b64_e32 v[222:223], 0\n" \
  "  v_rcp_f32_e32 v253, v200\n" \
  "  s_nop 4\n" \
  "  v_mul_f32_e32 v196, v196, v253\n" \
  "  v_mul_f32_e32 v197, v197, v253\n" \
  "  v_mul_f32_e32 v198, v198, v253\n" \
  "  v_mul_f32_e32 v199, v199, v253\n" \
  "  v_cvt_pk_bf16_f32 v196, v196, v197\n" \
  "  v_cvt_pk_bf16_f32 v197, v198, v199\n" \
  "  ds_write_b64 v182, v[196:197]\n" \
  "  s_waitcnt vmcnt(0) lgkmcnt(0)\n" \
  "  s_barrier\n" \
  "  ds_read_b128 v[192:195], v215 offset:59392\n" \
  "  ds_read_b128 v[204:207], v215 offset:60416\n" \
  "  s_waitcnt lgkmcnt(1)\n" \
  "  v_smfmac_f32_16x16x64_bf16 v[220:223], v[192:195], v[144:151], v186\n" \
  "  v_smfmac_f32_16x16x64_bf16 v[224:227], v[192:195], v[160:167], v186\n" \
  "  s_waitcnt lgkmcnt(0)\n" \
  "  v_smfmac_f32_16x16x64_bf16 v[220:223], v[204:207], v[152:159], v186\n" \
  "  v_smfmac_f32_16x16x64_bf16 v[224:227], v[204:207], v[168:175], v186\n" \
  "  s_nop 6\n" \
  "  v_cvt_pk_bf16_f32 v196, v220, v221\n" \
  "  v_cvt_pk_bf16_f32 v197, v222, v223\n" \
  "  v_bfi_b32 v196, 1, s29, v196\n" \
  "  v_cvt_pk_bf16_f32 v198, v224, v225\n" \
  "  v_cvt_pk_bf16_f32 v199, v226, v227\n" \
  "  v_bfi_b32 v198, 1, s29, v198\n" \
  "  buffer_store_dwordx4 v[196:199], v178, s[36:39], s41 offen sc1\n" \
  "  s_setprio 0\n" \
  "  v_add_u32_e32 v214, s45, v180\n" \
  "  ds_read_b128 v[192:195], v214 offset:26624\n" \
  "  ds_read_b128 v[200:203], v214 offset:28672\n" \
  "  ds_read_b128 v[204:207], v214 offset:30720\n" \
  "  ds_read_b128 v[216:219], v214 offset:32768\n" \
  "  ds_read_b128 v[220:223], v180 offset:10240\n" \
  "  ds_read_b128 v[196:199], v180 offset:12288\n" \
  "  ds_read_b128 v[252:255], v180 offset:14336\n" \
  "  s_waitcnt lgkmcnt(6)\n" \
  "  v_mfma_f32_16x16x32_bf16 v[232:235], v[236:239], v[192:195], 0\n" \
  "  ds_read_b128 v[236:239], v180 offset:16384\n" \
  "  s_waitcnt lgkmcnt(6)\n" \
  "  v_mfma_f32_16x16x32_bf16 v[232:235], v[240:243], v[200:203], v[232:235]\n" \
  "  ds_read_b128 v[240:243], v180 offset:12544\n" \
  "  s_waitcnt lgkmcnt(6)\n" \
  "  v_mfma_f32_16x16x32_bf16 v[232:235], v[244:247], v[204:207], v[232:235]\n" \
  "  ds_read_b128 v[244:247], v180 offset:14592\n" \
  "  s_waitcnt lgkmcnt(6)\n" \
  "  v_mfma_f32_16x16x32_bf16 v[232:235], v[248:251], v[216:219], v[232:235]\n" \
  "  ds_read_b128 v[248:251], v180 offset:16640\n" \
  "  s_waitcnt lgkmcnt(6)\n" \
  "  v_mfma_f32_16x16x32_bf16 v[224:227], v[220:223], v[192:195], 0\n" \
  "  ds_read_b128 v[220:223], v180 offset:10496\n" \
  "  s_waitcnt lgkmcnt(6)\n" \
  "  v_mfma_f32_16x16x32_bf16 v[224:227], v[196:199], v[200:203], v[224:227]\n" \
  "  s_waitcnt lgkmcnt(5)\n" \
  "  v_mfma_f32_16x16x32_bf16 v[224:227], v[252:255], v[204:207], v[224:227]\n" \
  "  s_waitcnt lgkmcnt(4)\n" \
  "  v_mfma_f32_16x16x32_bf16 v[224:227], v[236:239], v[216:219], v[224:227]\n" \
  "  s_waitcnt lgkmcnt(0)\n" \
  "  v_mfma_f32_16x16x32_bf16 v[228:231], v[220:223], v[192:195], 0\n" \
  "  v_mfma_f32_16x16x32_bf16 v[228:231], v[240:243], v[200:203], v[228:231]\n" \
  "  v_mfma_f32_16x16x32_bf16 v[228:231], v[244:247], v[204:207], v[228:231]\n" \
  "  v_mfma_f32_16x16x32_bf16 v[228:231], v[248:251], v[216:219], v[228:231]\n" \
  "  v_lshrrev_b32_e32 v249, 2, v176\n" \
  "  buffer_load_dwordx4 v[244:247], v176, s[36:39], s16 offen sc1\n" \
  "  buffer_load_dword v248, v249, s[36:39], s17 offen sc1\n" \
  "  v_xor_b32_e32 v214, s46, v181\n" \
  "  v_xor_b32_e32 v215, 0x400, v214\n" \
  "  ds_read_b64_tr_b16 v[236:237], v214 offset:0\n" \
  "  ds_read_b64_tr_b16 v[238:239], v214 offset:256\n" \
  "  ds_read_b64_tr_b16 v[240:241], v215 offset:0\n" \
  "  ds_read_b64_tr_b16 v[242:243], v215 offset:256\n" \
  "  v_mov_b32_e32 v254, 0xff800000\n" \
  "  v_cndmask_b32_e64 v224, v254, v224, s[50:51]\n" \
  "  v_cndmask_b32_e64 v225, v254, v225, s[52:53]\n" \
  "  v_cndmask_b32_e64 v226, v254, v226, s[54:55]\n" \
  "  v_cndmask_b32_e64 v227, v254, v227, s[56:57]\n" \
  "  v_cndmask_b32_e64 v228, v254, v228, s[58:59]\n" \
  "  v_cndmask_b32_e64 v229, v254, v229, s[60:61]\n" \
  "  v_cndmask_b32_e64 v230, v254, v230, s[62:63]\n" \
  "  v_cndmask_b32_e64 v231, v254, v231, s[64:65]\n" \
  "  v_max3_f32 v252, v232, v233, v224\n" \
  "  v_max3_f32 v252, v252, v225, v226\n" \
  "  v_max3_f32 v252, v252, v227, v228\n" \
  "  v_max3_f32 v252, v252, v229, v230\n" \
  "  v_max_f32_e32 v252, v252, v231\n" \
  "  v_mov_b32_e32 v253, v252\n" \
  "  s_nop 1\n" \
  "  v_permlane16_swap_b32_e32 v252, v253\n" \
  "  v_max_f32_e32 v252, v252, v253\n" \
  "  v_mov_b32_e32 v253, v252\n" \
  "  s_nop 1\n" \
  "  v_permlane32_swap_b32_e32 v252, v253\n" \
  "  v_max_f32_e32 v252, v252, v253\n" \
  "  v_mul_f32_e64 v252, v252, -s66\n" \
  "  v_fma_f32 v224, v224, s66, v252\n" \
  "  v_fma_f32 v225, v225, s66, v252\n" \
  "  v_fma_f32 v226, v226, s66, v252\n" \
  "  v_fma_f32 v227, v227, s66, v252\n" \
  "  v_fma_f32 v228, v228, s66, v252\n" \
  "  v_fma_f32 v229, v229, s66, v252\n" \
  "  v_fma_f32 v230, v230, s66, v252\n" \
  "  v_fma_f32 v231, v231, s66, v252\n" \
  "  v_fma_f32 v232, v232, s66, v252\n" \
  "  v_fma_f32 v233, v233, s66, v252\n" \
  "  v_exp_f32_e32 v224, v224\n" \
  "  v_exp_f32_e32 v225, v225\n" \
  "  v_exp_f32_e32 v226, v226\n" \
  "  v_exp_f32_e32 v227, v227\n" \
  "  v_exp_f32_e32 v228, v228\n" \
  "  v_exp_f32_e32 v229, v229\n" \
  "  v_exp_f32_e32 v230, v230\n" \
  "  v_exp_f32_e32 v231, v231\n" \
  "  v_exp_f32_e32 v232, v232\n" \
  "  v_exp_f32_e32 v233, v233\n" \
  "  v_cvt_pk_bf16_f32 v192, v224, v225\n" \
  "  v_cvt_pk_bf16_f32 v193, v226, v227\n" \
  "  v_cvt_pk_bf16_f32 v194, v228, v229\n" \
  "  v_cvt_pk_bf16_f32 v195, v230, v231\n" \
  "  v_cvt_pk_bf16_f32 v196, v232, v233\n" \
  "  v_mov_b32_e32 v197, 0\n" \
  "  v_mfma_f32_16x16x32_bf16 v[200:203], v[188:191], v[192:195], 0\n" \
  "  s_waitcnt lgkmcnt(2)\n" \
  "  v_mfma_f32_16x16x32_bf16 v[204:207], v[236:239], v[192:195], 0\n" \
  "  s_waitcnt lgkmcnt(0)\n" \
  "  v_mfma_f32_16x16x32_bf16 v[220:223], v[240:243], v[192:195], 0\n" \
  "  v_mov_b64_e32 v[228:229], 0\n" \
  "  v_mov_b64_e32 v[230:231], 0\n" \
  "  v_mov_b64_e32 v[232:233], 0\n" \
  "  v_mov_b64_e32 v[234:235], 0\n" \
  "  v_mfma_f32_16x16x16_bf16 v[200:203], v[188:189], v[196:197], v[200:203]\n" \
  "  v_mfma_f32_16x16x16_bf16 v[204:207], v[210:211], v[196:197], v[204:207]\n" \
  "  v_mfma_f32_16x16x16_bf16 v[220:223], v[212:213], v[196:197], v[220:223]\n" \
  "  v_add_u32_e32 v180, s43, v180\n" \
  "  v_add_u32_e32 v181, s44, v181\n" \
  "  s_add_u32 s19, s19, s43\n" \
  "  s_add_u32 s20, s20, s44\n" \
  "  v_add_u32_e32 v250, s19, v176\n" \
  "  v_add_u32_e32 v249, s20, v249\n" \
  "  s_add_u32 s16, s16, 262144\n" \
  "  s_add_u32 s17, s17, 262144\n" \
  "  s_sub_u32 s43, 0, s43\n" \
  "  s_sub_u32 s44, 0, s44\n" \
  "  s_sub_u32 s41, 65536, s41\n" \
  "  s_add_u32 s67, s67, 4\n" \
  "  v_rcp_f32_e32 v252, v200\n" \
  "  s_nop 0\n" \
  "  v_mul_f32_e32 v204, v204, v252\n" \
  "  v_mul_f32_e32 v205, v205, v252\n" \
  "  v_mul_f32_e32 v206, v206, v252\n" \
  "  v_mul_f32_e32 v207, v207, v252\n" \
  "  v_mul_f32_e32 v220, v220, v252\n" \
  "  v_mul_f32_e32 v221, v221, v252\n" \
  "  v_mul_f32_e32 v222, v222, v252\n" \
  "  v_mul_f32_e32 v223, v223, v252\n" \
  "  v_cvt_pk_bf16_f32 v224, v204, v205\n" \
  "  v_cvt_pk_bf16_f32 v225, v206, v207\n" \
  "  v_cvt_pk_bf16_f32 v226, v220, v221\n" \
  "  v_cvt_pk_bf16_f32 v227, v222, v223\n" \
  "  s_nop 0\n" \
  "  v_permlane16_swap_b32_e32 v224, v226\n" \
  "  v_permlane16_swap_b32_e32 v225, v227\n" \
  "  s_waitcnt vmcnt(1)\n" \
  "  ds_write_b128 v250, v[244:247]\n" \
  "  s_waitcnt vmcnt(0)\n" \
  "  ds_write_b32 v249, v248\n" \
  "  v_bfe_u32 v197, v176, 2, 7\n" \
  "  v_and_b32_e32 v198, 0x310, v176\n" \
  "  v_bfe_u32 v200, v176, 5, 2\n" \
  "  v_lshl_or_b32 v198, v200, 6, v198\n" \
  "  v_bfe_u32 v200, v176, 7, 1\n" \
  "  v_lshl_or_b32 v198, v200, 5, v198\n" \
  "  v_add_u32_e32 v198, s18, v198\n" \
  "  s_lshr_b32 s34, s18, 11\n" \
  "  s_lshl_b32 s34, s34, 8\n" \
  "  s_bfe_u32 s35, s18, 0x1000a\n" \
  "  s_lshl_b32 s35, s35, 5\n" \
  "  s_add_u32 s34, s34, s35\n" \
  "  v_and_b32_e32 v199, 0x1c, v197\n" \
  "  v_bfe_u32 v200, v197, 5, 2\n" \
  "  v_lshl_or_b32 v199, v200, 6, v199\n" \
  "  v_add_u32_e32 v199, s34, v199\n" \
  "  s_mov_b32 s25, 0\n" \
  "  s_lshl_b32 s42, s40, 18\n" \
  "  s_add_u32 s40, s40, 1\n" \
  "  s_bfe_u32 s29, s40, 0x10001\n" \
  "  s_and_b32 s22, s40, 3\n" \
  "  s_lshl_b32 s22, s22, 15\n" \
  "  s_add_u32 s23, s14, s22\n" \
  "  s_add_u32 s24, s15, s22\n" \
  "  s_cmp_lt_u32 s40, 64\n" \
  "  s_cbranch_scc1 LQ_%=\n" \
  "  buffer_store_dwordx4 v[224:227], v184, s[36:39], s42 offen sc1\n" \
  "  s_waitcnt vmcnt(0)\n" \
  "  s_mov_b64 exec, 1\n" \
  "  ds_inc_rtn_u32 v208, v176, v178 offset:9216\n" \
  "  s_mov_b64 exec, -1\n" \
  "  s_waitcnt lgkmcnt(0)\n" \
  "  v_readfirstlane_b32 s31, v208\n" \
  "  v_mov_b32_e32 v209, 63\n" \
  "  s_and_b32 s31, s31, 3\n" \
  "  s_cmp_eq_u32 s31, 3\n" \
  "  s_cbranch_scc0 LQob_%=\n" \
  "  s_mov_b64 exec, 1\n" \
  "  buffer_store_dword v209, off, s[36:39], s30 sc1\n" \
  "  s_mov_b64 exec, -1\n" \
  "LQob_%=:\n" \
  "  s_endpgm\n" \
  "Lhq_%=:\n" \
  "  s_waitcnt vmcnt(0) lgkmcnt(0)\n" \
  "  s_add_u32 s25, s25, 1\n" \
  "  s_max_u32 s25, s25, s26\n" \
  "  s_cmp_lt_u32 s25, 0x2000\n" \
  "  s_cbranch_scc0 Liq_%=\n" \
  "  buffer_load_dwordx4 v[192:195], v176, s[36:39], s23 offen sc1\n" \
  "  buffer_load_dword v196, v197, s[36:39], s24 offen sc1\n" \
  "  s_waitcnt vmcnt(1)\n" \
  "  ds_write_b128 v198, v[192:195] offset:0\n" \
  "  v_max3_u32 v200, v192, v193, v194\n" \
  "  s_waitcnt vmcnt(0)\n" \
  "  ds_write_b32 v199, v196 offset:8192\n" \
  "  v_max3_u32 v200, v200, v195, v196\n" \
  "  v_cmp_eq_u32_e32 vcc, -1, v200\n" \
  "  s_cbranch_vccnz Lhq_%=\n" \
  "  s_branch LQb_%=\n" \
  "Liq_%=:\n" \
  "  s_mov_b32 s26, 0x2000\n" \
  "  s_branch LQb_%=\n" \
  ".Lfkq1:\n"

#define CHAIN_K_ASM \
  ".Lfkk0:\n" \
  "  s_mov_b32 s19, 2\n" \
  "  s_mov_b32 s64, 0\n" \
  "  s_mov_b32 s18, 4294959104\n" \
  "  s_mov_b32 s14, 4294963200\n" \
  "  s_mov_b32 s40, 0\n" \
  "  s_mov_b32 s41, 0\n" \
  "  s_mov_b32 s42, 0\n" \
  "  s_mov_b32 s65, 0\n" \
  "  s_mov_b32 s71, 4\n" \
  "  s_mov_b32 s30, 0x100000\n" \
  "  s_mov_b32 s49, 0\n" \
  "  v_mov_b64_e32 v[212:213], 0\n" \
  "  v_mov_b64_e32 v[214:215], 0\n" \
  "  v_mov_b64_e32 v[216:217], 0\n" \
  "  v_mov_b64_e32 v[218:219], 0\n" \
  "  s_waitcnt lgkmcnt(0)\n" \
  "  s_barrier\n" \
  "  ds_read_b128 v[220:223], v166 offset:0\n" \
  "  ds_read_b128 v[224:227], v166 offset:4096\n" \
  "  ds_read_b128 v[228:231], v166 offset:8192\n" \
  "  ds_read_b128 v[232:235], v166 offset:12288\n" \
  "  ds_read_b128 v[236:239], v166 offset:16384\n" \
  "  ds_read_b128 v[240:243], v166 offset:20480\n" \
  "  ds_read_b128 v[244:247], v166 offset:24576\n" \
  "  ds_read_b128 v[248:251], v166 offset:28672\n" \
  "  s_memrealtime s[68:69]\n" \
  "  s_waitcnt vmcnt(0) lgkmcnt(0)\n" \
  "  s_mov_b32 s28, s68\n" \
  "  s_branch LKb_%=\n" \
  "LK_%=:\n" \
  "  s_barrier\n" \
  "  buffer_load_dwordx4 v[176:179], v160, s[36:39], s23 offen sc1\n" \
  "  buffer_load_dword v180, v181, s[36:39], s24 offen sc1\n" \
  "  s_waitcnt vmcnt(1)\n" \
  "  ds_write_b128 v182, v[176:179] offset:0\n" \
  "  v_max3_u32 v184, v176, v177, v178\n" \
  "  s_waitcnt vmcnt(0)\n" \
  "  ds_write_b32 v183, v180 offset:8192\n" \
  "  v_max3_u32 v184, v184, v179, v180\n" \
  "  v_cmp_eq_u32_e32 vcc, -1, v184\n" \
  "  s_cbranch_vccnz Lhk_%=\n" \
  "LKb_%=:\n" \
  "  s_waitcnt lgkmcnt(0)\n" \
  "  s_barrier\n" \
  "  s_setprio 2\n" \
  "  ds_read_b128 v[176:179], v160 offset:0\n" \
  "  ds_read_b128 v[180:183], v160 offset:1024\n" \
  "  ds_read_b128 v[184:187], v160 offset:2048\n" \
  "  ds_read_b128 v[188:191], v160 offset:3072\n" \
  "  ds_read_b128 v[192:195], v160 offset:4096\n" \
  "  ds_read_b128 v[196:199], v160 offset:5120\n" \
  "  ds_read_b128 v[200:203], v160 offset:6144\n" \
  "  ds_read_b128 v[204:207], v160 offset:7168\n" \
  "  ds_read_b128 v[208:211], v160 offset:8192\n" \
  "  s_waitcnt lgkmcnt(8)\n" \
  "  v_smfmac_f32_16x16x64_bf16 v[212:215], v[176:179], v[0:7], v161\n" \
  "  v_smfmac_f32_16x16x64_bf16 v[216:219], v[176:179], v[8:15], v161\n" \
  "  s_waitcnt lgkmcnt(7)\n" \
  "  v_smfmac_f32_16x16x64_bf16 v[212:215], v[180:183], v[16:23], v161\n" \
  "  v_smfmac_f32_16x16x64_bf16 v[216:219], v[180:183], v[24:31], v161\n" \
  "  s_waitcnt lgkmcnt(6)\n" \
  "  v_smfmac_f32_16x16x64_bf16 v[212:215], v[184:187], v[32:39], v161\n" \
  "  v_smfmac_f32_16x16x64_bf16 v[216:219], v[184:187], v[40:47], v161\n" \
  "  s_waitcnt lgkmcnt(5)\n" \
  "  v_smfmac_f32_16x16x64_bf16 v[212:215], v[188:191], v[48:55], v161\n" \
  "  v_smfmac_f32_16x16x64_bf16 v[216:219], v[188:191], v[56:63], v161\n" \
  "  s_waitcnt lgkmcnt(4)\n" \
  "  v_smfmac_f32_16x16x64_bf16 v[212:215], v[192:195], v[64:71], v161\n" \
  "  v_smfmac_f32_16x16x64_bf16 v[216:219], v[192:195], v[72:79], v161\n" \
  "  s_waitcnt lgkmcnt(3)\n" \
  "  v_smfmac_f32_16x16x64_bf16 v[212:215], v[196:199], v[80:87], v161\n" \
  "  v_smfmac_f32_16x16x64_bf16 v[216:219], v[196:199], v[88:95], v161\n" \
  "  v_mov_b64_e32 v[252:253], 0\n" \
  "  v_mov_b64_e32 v[254:255], 0\n" \
  "  s_waitcnt lgkmcnt(2)\n" \
  "  v_smfmac_f32_16x16x64_bf16 v[212:215], v[200:203], v[96:103], v161\n" \
  "  v_smfmac_f32_16x16x64_bf16 v[216:219], v[200:203], v[104:111], v161\n" \
  "  s_waitcnt lgkmcnt(1)\n" \
  "  v_smfmac_f32_16x16x64_bf16 v[212:215], v[204:207], v[112:119], v161\n" \
  "  v_smfmac_f32_16x16x64_bf16 v[216:219], v[204:207], v[120:127], v161\n" \
  "  s_barrier\n" \
  "  s_waitcnt lgkmcnt(0)\n" \
  "  v_smfmac_f32_16x16x64_bf16 v[212:215], v[208:211], v[128:135], v161\n" \
  "  v_smfmac_f32_16x16x64_bf16 v[216:219], v[208:211], v[136:143], v161\n" \
  "  v_smfmac_f32_16x16x64_bf16 v[252:255], v[176:179], v[220:227], v161\n" \
  "  v_smfmac_f32_16x16x64_bf16 v[252:255], v[180:183], v[228:235], v161\n" \
  "  s_nop 4\n" \
  "  v_add_f32_e32 v212, v212, v214\n" \
  "  v_add_f32_e32 v213, v213, v215\n" \
  "  v_add_f32_e32 v216, v216, v218\n" \
  "  v_add_f32_e32 v217, v217, v219\n" \
  "  v_cvt_pk_bf16_f32 v172, v212, v213\n" \
  "  v_cvt_pk_bf16_f32 v173, v216, v217\n" \
  "  ds_write2_b32 v168, v172, v173 offset1:128\n" \
  "  s_setprio 0\n" \
  "  ds_read_b128 v[220:223], v166 offset:32768\n" \
  "  ds_read_b128 v[224:227], v166 offset:36864\n" \
  "  ds_read_b128 v[228:231], v166 offset:40960\n" \
  "  ds_read_b128 v[232:235], v166 offset:45056\n" \
  "  v_smfmac_f32_16x16x64_bf16 v[252:255], v[184:187], v[236:243], v161\n" \
  "  ds_read_b128 v[236:239], v166 offset:49152\n" \
  "  ds_read_b128 v[240:243], v166 offset:53248\n" \
  "  v_smfmac_f32_16x16x64_bf16 v[252:255], v[188:191], v[244:251], v161\n" \
  "  v_add_u32_e32 v170, 0x8000, v166\n" \
  "  ds_read_b128 v[244:247], v170 offset:24576\n" \
  "  ds_read_b128 v[248:251], v170 offset:28672\n" \
  "  ds_read_b128 v[212:215], v170 offset:32768\n" \
  "  ds_read_b128 v[216:219], v170 offset:36864\n" \
  "  s_waitcnt lgkmcnt(10)\n" \
  "  s_barrier\n" \
  "  v_mov_b32_e32 v173, 0\n" \
  "  s_waitcnt lgkmcnt(8)\n" \
  "  v_smfmac_f32_16x16x64_bf16 v[252:255], v[192:195], v[220:227], v161\n" \
  "  s_waitcnt lgkmcnt(6)\n" \
  "  v_smfmac_f32_16x16x64_bf16 v[252:255], v[196:199], v[228:235], v161\n" \
  "  s_waitcnt lgkmcnt(4)\n" \
  "  v_smfmac_f32_16x16x64_bf16 v[252:255], v[200:203], v[236:243], v161\n" \
  "  s_waitcnt lgkmcnt(2)\n" \
  "  v_smfmac_f32_16x16x64_bf16 v[252:255], v[204:207], v[244:251], v161\n" \
  "  s_waitcnt lgkmcnt(0)\n" \
  "  v_smfmac_f32_16x16x64_bf16 v[252:255], v[208:211], v[212:219], v161\n" \
  "  v_add_u32_e32 v170, s50, v160\n" \
  "  v_add_u32_e32 v171, s25, v160\n" \
  "  ds_read_b128 v[220:223], v170 offset:0\n" \
  "  ds_read_b128 v[224:227], v170 offset:1024\n" \
  "  ds_read_b128 v[228:231], v170 offset:2048\n" \
  "  ds_read_b128 v[232:235], v170 offset:3072\n" \
  "  s_nop 1\n" \
  "  v_add_f32_e32 v252, v252, v254\n" \
  "  v_add_f32_e32 v253, v253, v255\n" \
  "  v_cvt_pk_bf16_f32 v172, v252, v253\n" \
  "  ds_write_b64 v169, v[172:173]\n" \
  "  ds_inc_u32 v160, v162 offset:62472\n" \
  "  s_setprio 0\n" \
  "  s_cmp_eq_u32 s40, 63\n" \
  "  s_cbranch_scc1 LKe_%=\n" \
  "  s_cmp_eq_u32 s48, 3\n" \
  "  s_cbranch_scc1 LKr_%=\n" \
  "  ds_read_b128 v[236:239], v171 offset:0\n" \
  "  ds_read_b128 v[240:243], v171 offset:1024\n" \
  "  ds_read_b128 v[244:247], v171 offset:2048\n" \
  "  ds_read_b128 v[248:251], v171 offset:3072\n" \
  "  s_cmp_ge_i32 s16, s19\n" \
  "  s_cbranch_scc1 LKxs_%=\n" \
  "  s_mov_b32 s62, s64\n" \
  "LKys_%=:\n" \
  "  buffer_load_dword v252, off, s[36:39], s17 offset:0 sc1\n" \
  "  buffer_load_dword v253, off, s[36:39], s17 offset:256 sc1\n" \
  "  buffer_load_dword v254, off, s[36:39], s17 offset:512 sc1\n" \
  "  s_waitcnt vmcnt(0)\n" \
  "  v_min3_i32 v252, v252, v253, v254\n" \
  "  s_nop 0\n" \
  "  v_readfirstlane_b32 s16, v252\n" \
  "  s_nop 0\n" \
  "  s_add_u32 s16, s16, 1\n" \
  "  s_add_u32 s62, s62, 1\n" \
  "  s_cmp_ge_i32 s16, s19\n" \
  "  s_cselect_b32 s62, 0x2000, s62\n" \
  "  s_cmp_lt_u32 s62, 0x2000\n" \
  "  s_cbranch_scc1 LKys_%=\n" \
  "  s_cmp_ge_i32 s16, s19\n" \
  "  s_cselect_b32 s64, s64, 0x2000\n" \
  "LKxs_%=:\n" \
  "  s_add_u32 s62, s66, s63\n" \
  "  s_sub_u32 s67, s66, 16777216\n" \
  "  buffer_load_dwordx4 v[176:179], v160, s[36:39], s66 offen sc1\n" \
  "  buffer_load_dwordx4 v[180:183], v160, s[36:39], s66 offen offset:1024 sc1\n" \
  "  buffer_load_dwordx4 v[184:187], v160, s[36:39], s62 offen sc1\n" \
  "  buffer_load_dwordx4 v[188:191], v160, s[36:39], s67 offen sc1\n" \
  "  buffer_load_dwordx4 v[192:195], v160, s[36:39], s67 offen offset:1024 sc1\n" \
  "  s_add_u32 s66, s66, 262144\n" \
  "  s_add_u32 s62, s19, 1\n" \
  "  s_min_i32 s62, s62, 64\n" \
  "  s_cmp_lt_i32 s16, s62\n" \
  "  s_cselect_b32 s15, 1, 0\n" \
  "  s_cbranch_scc0 LKf_%=\n" \
  "  buffer_load_dword v252, off, s[36:39], s17 offset:0 sc1\n" \
  "  buffer_load_dword v253, off, s[36:39], s17 offset:256 sc1\n" \
  "  buffer_load_dword v254, off, s[36:39], s17 offset:512 sc1\n" \
  "LKf_%=:\n" \
  "  s_add_u32 s62, s40, 3\n" \
  "  s_bfe_u32 s62, s62, 0x10001\n" \
  "  s_sub_u32 s67, 65536, s41\n" \
  "  s_mov_b32 s53, 4294443008\n" \
  "  v_mov_b32_e32 v216, s62\n" \
  "  v_mov_b32_e32 v218, s62\n" \
  "  v_xor_b32_e32 v217, 0x4000, v162\n" \
  "  v_add_u32_e32 v217, s53, v217\n" \
  "  buffer_store_dwordx4 v[216:219], v162, s[36:39], s67 offen sc1\n" \
  "  buffer_store_dwordx4 v[216:219], v217, s[36:39], s67 offen sc1\n" \
  "  s_cmp_lt_u32 s48, 2\n" \
  "  s_cbranch_scc0 LKpb_%=\n" \
  "  buffer_store_dwordx2 v[216:217], v165, s[36:39], s67 offen sc1\n" \
  "LKpb_%=:\n" \
  "  s_add_u32 s53, s42, 32768\n" \
  "  s_and_b32 s53, s53, 131071\n" \
  "  s_add_u32 s23, s20, s53\n" \
  "  s_add_u32 s24, s21, s53\n" \
  "  s_mov_b32 s52, s42\n" \
  "  s_mov_b32 s42, s53\n" \
  "  s_mov_b32 s65, 0\n" \
  "  v_mov_b64_e32 v[204:205], 0\n" \
  "  v_mov_b64_e32 v[206:207], 0\n" \
  "  v_mov_b64_e32 v[208:209], 0\n" \
  "  v_mov_b64_e32 v[210:211], 0\n" \
  "  v_mov_b64_e32 v[212:213], 0\n" \
  "  v_mov_b64_e32 v[214:215], 0\n" \
  "  s_setprio 3\n" \
  "  s_barrier\n" \
  "  ds_read_b128 v[196:199], v164 offset:59392\n" \
  "  ds_read_b128 v[200:203], v164 offset:60416\n" \
  "  s_waitcnt lgkmcnt(1)\n" \
  "  v_smfmac_f32_16x16x64_bf16 v[204:207], v[196:199], v[144:151], v167\n" \
  "  v_smfmac_f32_16x16x64_bf16 v[208:211], v[196:199], v[220:227], v167\n" \
  "  s_waitcnt lgkmcnt(0)\n" \
  "  v_smfmac_f32_16x16x64_bf16 v[204:207], v[200:203], v[152:159], v167\n" \
  "  v_smfmac_f32_16x16x64_bf16 v[208:211], v[200:203], v[228:235], v167\n" \
  "  s_cmp_lt_u32 s48, 2\n" \
  "  s_cbranch_scc0 LKw_%=\n" \
  "  v_smfmac_f32_16x16x64_bf16 v[212:215], v[196:199], v[236:243], v167\n" \
  "  v_smfmac_f32_16x16x64_bf16 v[212:215], v[200:203], v[244:251], v167\n" \
  "  s_nop 2\n" \
  "  v_cvt_pk_bf16_f32 v216, v204, v205\n" \
  "  v_cvt_pk_bf16_f32 v217, v206, v207\n" \
  "  v_bfi_b32 v216, 1, s49, v216\n" \
  "  v_cvt_pk_bf16_f32 v218, v208, v209\n" \
  "  v_cvt_pk_bf16_f32 v219, v210, v211\n" \
  "  v_bfi_b32 v218, 1, s49, v218\n" \
  "  buffer_store_dwordx4 v[216:219], v162, s[36:39], s41 offen sc1\n" \
  "  v_cvt_pk_bf16_f32 v212, v212, v213\n" \
  "  v_cvt_pk_bf16_f32 v213, v214, v215\n" \
  "  v_bfi_b32 v212, 1, s49, v212\n" \
  "  buffer_store_dwordx2 v[212:213], v165, s[36:39], s41 offen sc1\n" \
  "  s_branch LKv_%=\n" \
  "LKw_%=:\n" \
  "  s_nop 4\n" \
  "  v_cvt_pk_bf16_f32 v216, v204, v205\n" \
  "  v_cvt_pk_bf16_f32 v217, v206, v207\n" \
  "  v_bfi_b32 v216, 1, s49, v216\n" \
  "  v_cvt_pk_bf16_f32 v218, v208, v209\n" \
  "  v_cvt_pk_bf16_f32 v219, v210, v211\n" \
  "  v_bfi_b32 v218, 1, s49, v218\n" \
  "  buffer_store_dwordx4 v[216:219], v162, s[36:39], s41 offen sc1\n" \
  "LKv_%=:\n" \
  "  s_setprio 0\n" \
  "  s_cmp_eq_u32 s31, 0\n" \
  "  s_cbranch_scc1 LKos_%=\n" \
  "  v_mov_b32_e32 v172, s40\n" \
  "  v_add_u32_e32 v172, -1, v172\n" \
  "  s_mov_b64 exec, 1\n" \
  "  buffer_store_dword v172, off, s[36:39], s31 sc1\n" \
  "  s_mov_b64 exec, -1\n" \
  "LKos_%=:\n" \
  "  s_waitcnt vmcnt(1)\n" \
  "  s_add_u32 s19, s40, 3\n" \
  "  v_add_u32_e32 v172, s43, v160\n" \
  "  v_add_u32_e32 v173, s44, v160\n" \
  "  ds_write_b128 v172, v[176:179]\n" \
  "  ds_write_b128 v172, v[180:183] offset:1024\n" \
  "  ds_write_b128 v173, v[184:187]\n" \
  "  ds_write_b128 v172, v[188:191] offset:16384\n" \
  "  ds_write_b128 v172, v[192:195] offset:17408\n" \
  "  s_add_u32 s43, s43, s18\n" \
  "  s_add_u32 s44, s44, s14\n" \
  "  s_sub_u32 s18, 0, s18\n" \
  "  s_sub_u32 s14, 0, s14\n" \
  "  s_cmp_eq_u32 s15, 1\n" \
  "  s_cbranch_scc0 LKcg_%=\n" \
  "  v_min3_i32 v252, v252, v253, v254\n" \
  "  s_nop 0\n" \
  "  v_readfirstlane_b32 s16, v252\n" \
  "  s_nop 0\n" \
  "  s_add_u32 s16, s16, 1\n" \
  "LKcg_%=:\n" \
  "  s_sub_u32 s41, 65536, s41\n" \
  "  s_add_u32 s40, s40, 1\n" \
  "  s_add_u32 s71, s71, 4\n" \
  "  s_bfe_u32 s49, s40, 0x10001\n" \
  "  v_mov_b64_e32 v[212:213], 0\n" \
  "  v_mov_b64_e32 v[214:215], 0\n" \
  "  v_mov_b64_e32 v[216:217], 0\n" \
  "  v_mov_b64_e32 v[218:219], 0\n" \
  "  v_bfe_u32 v181, v160, 2, 7\n" \
  "  v_and_b32_e32 v182, 0x310, v160\n" \
  "  v_bfe_u32 v184, v160, 5, 2\n" \
  "  v_lshl_or_b32 v182, v184, 6, v182\n" \
  "  v_bfe_u32 v184, v160, 7, 1\n" \
  "  v_lshl_or_b32 v182, v184, 5, v182\n" \
  "  v_add_u32_e32 v182, s22, v182\n" \
  "  s_lshr_b32 s34, s22, 11\n" \
  "  s_lshl_b32 s34, s34, 8\n" \
  "  s_bfe_u32 s35, s22, 0x1000a\n" \
  "  s_lshl_b32 s35, s35, 5\n" \
  "  s_add_u32 s34, s34, s35\n" \
  "  v_and_b32_e32 v183, 0x1c, v181\n" \
  "  v_bfe_u32 v184, v181, 5, 2\n" \
  "  v_lshl_or_b32 v183, v184, 6, v183\n" \
  "  v_add_u32_e32 v183, s34, v183\n" \
  "  s_waitcnt lgkmcnt(0)\n" \
  "  ds_read_b128 v[220:223], v166 offset:0\n" \
  "  ds_read_b128 v[224:227], v166 offset:4096\n" \
  "  ds_read_b128 v[228:231], v166 offset:8192\n" \
  "  ds_read_b128 v[232:235], v166 offset:12288\n" \
  "  ds_read_b128 v[236:239], v166 offset:16384\n" \
  "  ds_read_b128 v[240:243], v166 offset:20480\n" \
  "  ds_read_b128 v[244:247], v166 offset:24576\n" \
  "  ds_read_b128 v[248:251], v166 offset:28672\n" \
  "  s_branch LK_%=\n" \
  "Lhk_%=:\n" \
  "  s_waitcnt vmcnt(0) lgkmcnt(0)\n" \
  "  s_add_u32 s65, s65, 1\n" \
  "  s_max_u32 s65, s65, s64\n" \
  "  s_cmp_lt_u32 s65, 0x2000\n" \
  "  s_cbranch_scc0 Lik_%=\n" \
  "  buffer_load_dwordx4 v[176:179], v160, s[36:39], s23 offen sc1\n" \
  "  buffer_load_dword v180, v181, s[36:39], s24 offen sc1\n" \
  "  s_waitcnt vmcnt(1)\n" \
  "  ds_write_b128 v182, v[176:179] offset:0\n" \
  "  v_max3_u32 v184, v176, v177, v178\n" \
  "  s_waitcnt vmcnt(0)\n" \
  "  ds_write_b32 v183, v180 offset:8192\n" \
  "  v_max3_u32 v184, v184, v179, v180\n" \
  "  v_cmp_eq_u32_e32 vcc, -1, v184\n" \
  "  s_cbranch_vccnz Lhk_%=\n" \
  "  s_branch LKb_%=\n" \
  "Lik_%=:\n" \
  "  s_mov_b32 s64, 0x2000\n" \
  "  s_branch LKb_%=\n" \
  "LKr_%=:\n" \
  "  s_and_b32 s66, s40, 3\n" \
  "  s_lshl_b32 s66, s66, 15\n" \
  "  s_add_u32 s66, s66, s51\n" \
  "  buffer_load_dwordx2 v[174:175], off, s[36:39], s66 sc1\n" \
  "  s_add_u32 s62, s40, 3\n" \
  "  s_bfe_u32 s62, s62, 0x10001\n" \
  "  s_sub_u32 s67, 65536, s41\n" \
  "  s_mov_b32 s53, 4294443008\n" \
  "  v_mov_b32_e32 v216, s62\n" \
  "  v_mov_b32_e32 v218, s62\n" \
  "  v_xor_b32_e32 v217, 0x4000, v162\n" \
  "  v_add_u32_e32 v217, s53, v217\n" \
  "  buffer_store_dwordx4 v[216:219], v162, s[36:39], s67 offen sc1\n" \
  "  buffer_store_dwordx4 v[216:219], v217, s[36:39], s67 offen sc1\n" \
  "  s_add_u32 s53, s42, 32768\n" \
  "  s_and_b32 s53, s53, 131071\n" \
  "  s_add_u32 s23, s20, s53\n" \
  "  s_add_u32 s24, s21, s53\n" \
  "  s_mov_b32 s52, s42\n" \
  "  s_mov_b32 s42, s53\n" \
  "  s_mov_b32 s65, 0\n" \
  "  s_add_u32 s27, s53, 16384\n" \
  "  s_add_u32 s54, s46, s41\n" \
  "  s_mov_b32 s47, s49\n" \
  "  s_add_u32 s55, s54, 1024\n" \
  "  s_add_u32 s56, s54, 2048\n" \
  "  s_add_u32 s57, s54, 3072\n" \
  "  s_add_u32 s58, s54, 32768\n" \
  "  s_add_u32 s59, s54, 33792\n" \
  "  s_add_u32 s60, s54, 34816\n" \
  "  s_add_u32 s61, s54, 35840\n" \
  "  v_bfe_u32 v181, v160, 2, 7\n" \
  "  v_and_b32_e32 v182, 0x310, v160\n" \
  "  v_bfe_u32 v184, v160, 5, 2\n" \
  "  v_lshl_or_b32 v182, v184, 6, v182\n" \
  "  v_bfe_u32 v184, v160, 7, 1\n" \
  "  v_lshl_or_b32 v182, v184, 5, v182\n" \
  "  v_add_u32_e32 v182, s22, v182\n" \
  "  s_lshr_b32 s34, s22, 11\n" \
  "  s_lshl_b32 s34, s34, 8\n" \
  "  s_bfe_u32 s35, s22, 0x1000a\n" \
  "  s_lshl_b32 s35, s35, 5\n" \
  "  s_add_u32 s34, s34, s35\n" \
  "  v_and_b32_e32 v183, 0x1c, v181\n" \
  "  v_bfe_u32 v184, v181, 5, 2\n" \
  "  v_lshl_or_b32 v183, v184, 6, v183\n" \
  "  v_add_u32_e32 v183, s34, v183\n" \
  "  v_mov_b64_e32 v[204:205], 0\n" \
  "  v_mov_b64_e32 v[206:207], 0\n" \
  "  v_mov_b64_e32 v[208:209], 0\n" \
  "  v_mov_b64_e32 v[210:211], 0\n" \
  "  s_setprio 3\n" \
  "  s_barrier\n" \
  "  ds_read_b128 v[196:199], v164 offset:59392\n" \
  "  ds_read_b128 v[200:203], v164 offset:60416\n" \
  "  s_waitcnt lgkmcnt(1)\n" \
  "  v_smfmac_f32_16x16x64_bf16 v[204:207], v[196:199], v[144:151], v167\n" \
  "  v_smfmac_f32_16x16x64_bf16 v[208:211], v[196:199], v[220:227], v167\n" \
  "  s_waitcnt lgkmcnt(0)\n" \
  "  v_smfmac_f32_16x16x64_bf16 v[204:207], v[200:203], v[152:159], v167\n" \
  "  v_smfmac_f32_16x16x64_bf16 v[208:211], v[200:203], v[228:235], v167\n" \
  "  s_memrealtime s[68:69]\n" \
  "  s_nop 5\n" \
  "  v_cvt_pk_bf16_f32 v216, v204, v205\n" \
  "  v_cvt_pk_bf16_f32 v217, v206, v207\n" \
  "  v_bfi_b32 v216, 1, s49, v216\n" \
  "  v_cvt_pk_bf16_f32 v218, v208, v209\n" \
  "  v_cvt_pk_bf16_f32 v219, v210, v211\n" \
  "  v_bfi_b32 v218, 1, s49, v218\n" \
  "  buffer_store_dwordx4 v[216:219], v162, s[36:39], s41 offen sc1\n" \
  "  s_setprio 3\n" \
  "  s_waitcnt vmcnt(3)\n" \
  "  v_readfirstlane_b32 s62, v174\n" \
  "  v_readfirstlane_b32 s67, v175\n" \
  "  s_nop 0\n" \
  "  s_cmp_eq_u32 s67, s40\n" \
  "  s_cbranch_scc0 LKs_%=\n" \
  "LKt_%=:\n" \
  "  s_mov_b32 s28, s62\n" \
  "LKu_%=:\n" \
  "  ds_read_b128 v[220:223], v166 offset:0\n" \
  "  ds_read_b128 v[224:227], v166 offset:4096\n" \
  "  ds_read_b128 v[228:231], v166 offset:8192\n" \
  "  ds_read_b128 v[232:235], v166 offset:12288\n" \
  "  ds_read_b128 v[236:239], v166 offset:16384\n" \
  "  ds_read_b128 v[240:243], v166 offset:20480\n" \
  "  ds_read_b128 v[244:247], v166 offset:24576\n" \
  "  ds_read_b128 v[248:251], v166 offset:28672\n" \
  "  v_mov_b64_e32 v[174:175], -1\n" \
  "  ds_read_b128 v[216:219], v160 offset:61440\n" \
  "  s_sub_u32 s41, 65536, s41\n" \
  "  s_add_u32 s40, s40, 1\n" \
  "  s_add_u32 s71, s71, 4\n" \
  "  s_bfe_u32 s49, s40, 0x10001\n" \
  "  s_waitcnt lgkmcnt(0)\n" \
  "Ldr_%=:\n" \
  "  s_sub_u32 s62, s68, s28\n" \
  "  s_memrealtime s[68:69]\n" \
  "  s_add_u32 s62, s62, 2000\n" \
  "  s_cmp_lt_u32 s62, 1997\n" \
  "  s_cbranch_scc1 Ldr_%=\n" \
  "  s_cmp_lt_u32 s62, 2000\n" \
  "  s_cbranch_scc0 Ler_%=\n" \
  "Lfr_%=:\n" \
  "  s_add_u32 s62, s62, 1\n" \
  "  s_cmp_lt_u32 s62, 2000\n" \
  "  s_cbranch_scc1 Lfr_%=\n" \
  "Ler_%=:\n" \
  "Llk_%=:\n" \
  "  buffer_load_dwordx4 v[184:187], v160, s[36:39], s54 offen sc1\n" \
  "  buffer_load_dwordx4 v[188:191], v160, s[36:39], s55 offen sc1\n" \
  "  buffer_load_dwordx4 v[192:195], v160, s[36:39], s56 offen sc1\n" \
  "  buffer_load_dwordx4 v[196:199], v160, s[36:39], s57 offen sc1\n" \
  "  buffer_load_dwordx4 v[200:203], v160, s[36:39], s58 offen sc1\n" \
  "  buffer_load_dwordx4 v[204:207], v160, s[36:39], s59 offen sc1\n" \
  "  buffer_load_dwordx4 v[208:211], v160, s[36:39], s60 offen sc1\n" \
  "  buffer_load_dwordx4 v[212:215], v160, s[36:39], s61 offen sc1\n" \
  "  s_waitcnt vmcnt(7)\n" \
  "  v_mfma_f32_16x16x32_bf16 v[176:179], v[184:187], v[216:219], 0\n" \
  "  v_bitop3_b32 v253, v184, v186, s47 bitop3:0x7e\n" \
  "  s_waitcnt vmcnt(6)\n" \
  "  v_mfma_f32_16x16x32_bf16 v[176:179], v[188:191], v[216:219], v[176:179]\n" \
  "  v_bitop3_b32 v254, v188, v190, s47 bitop3:0x7e\n" \
  "  v_or_b32_e32 v252, v253, v254\n" \
  "  s_waitcnt vmcnt(5)\n" \
  "  v_mfma_f32_16x16x32_bf16 v[176:179], v[192:195], v[216:219], v[176:179]\n" \
  "  v_bitop3_b32 v253, v192, v194, s47 bitop3:0x7e\n" \
  "  s_waitcnt vmcnt(4)\n" \
  "  v_mfma_f32_16x16x32_bf16 v[176:179], v[196:199], v[216:219], v[176:179]\n" \
  "  v_bitop3_b32 v254, v196, v198, s47 bitop3:0x7e\n" \
  "  v_or3_b32 v252, v252, v253, v254\n" \
  "  s_waitcnt vmcnt(3)\n" \
  "  v_mfma_f32_16x16x32_bf16 v[176:179], v[200:203], v[216:219], v[176:179]\n" \
  "  v_bitop3_b32 v253, v200, v202, s47 bitop3:0x7e\n" \
  "  s_waitcnt vmcnt(2)\n" \
  "  v_mfma_f32_16x16x32_bf16 v[176:179], v[204:207], v[216:219], v[176:179]\n" \
  "  v_bitop3_b32 v254, v204, v206, s47 bitop3:0x7e\n" \
  "  v_or3_b32 v252, v252, v253, v254\n" \
  "  s_waitcnt vmcnt(1)\n" \
  "  v_mfma_f32_16x16x32_bf16 v[176:179], v[208:211], v[216:219], v[176:179]\n" \
  "  v_bitop3_b32 v253, v208, v210, s47 bitop3:0x7e\n" \
  "  s_waitcnt vmcnt(0)\n" \
  "  v_mfma_f32_16x16x32_bf16 v[176:179], v[212:215], v[216:219], v[176:179]\n" \
  "  v_bitop3_b32 v254, v212, v214, s47 bitop3:0x7e\n" \
  "  v_or3_b32 v252, v252, v253, v254\n" \
  "  v_and_b32_e32 v252, 1, v252\n" \
  "  v_cmp_ne_u32_e32 vcc, 0, v252\n" \
  "  s_nop 3\n" \
  "  v_permlane32_swap_b32_e32 v176, v178\n" \
  "  v_permlane32_swap_b32_e32 v177, v179\n" \
  "  v_add_f32_e32 v176, v176, v178\n" \
  "  v_add_f32_e32 v177, v177, v179\n" \
  "  s_cbranch_vccnz Lnk_%=\n" \
  "Lpk_%=:\n" \
  "  s_memrealtime s[68:69]\n" \
  "  v_cvt_pk_bf16_f32 v172, v176, v177\n" \
  "  v_min_u32_e32 v172, -2, v172\n" \
  "  v_mov_b32_e32 v173, v172\n" \
  "  s_nop 1\n" \
  "  v_permlane32_swap_b32_e32 v172, v173\n" \
  "  buffer_store_dwordx2 v[172:173], v163, s[36:39], s53 offen sc1\n" \
  "  buffer_store_dwordx2 v[172:173], v163, s[36:39], s27 offen sc1\n" \
  "  buffer_store_dwordx2 v[174:175], v163, s[36:39], s52 offen sc1\n" \
  "  s_add_u32 s27, s52, 16384\n" \
  "  buffer_store_dwordx2 v[174:175], v163, s[36:39], s27 offen sc1\n" \
  "  v_mov_b64_e32 v[212:213], 0\n" \
  "  v_mov_b64_e32 v[214:215], 0\n" \
  "  v_mov_b64_e32 v[216:217], 0\n" \
  "  v_mov_b64_e32 v[218:219], 0\n" \
  "  s_add_u32 s28, s28, s29\n" \
  "  s_mov_b32 s65, 0\n" \
  "Ldg_%=:\n" \
  "  s_sub_u32 s62, s68, s28\n" \
  "  s_memrealtime s[68:69]\n" \
  "  s_add_u32 s62, s62, 2000\n" \
  "  s_cmp_lt_u32 s62, 1997\n" \
  "  s_cbranch_scc1 Ldg_%=\n" \
  "  s_cmp_lt_u32 s62, 2000\n" \
  "  s_cbranch_scc0 Leg_%=\n" \
  "Lfg_%=:\n" \
  "  s_add_u32 s62, s62, 1\n" \
  "  s_cmp_lt_u32 s62, 2000\n" \
  "  s_cbranch_scc1 Lfg_%=\n" \
  "Leg_%=:\n" \
  "  s_branch LK_%=\n" \
  "Lnk_%=:\n" \
  "  s_add_u32 s65, s65, 1\n" \
  "  s_max_u32 s65, s65, s64\n" \
  "  s_cmp_lt_u32 s65, 0x2000\n" \
  "  s_cbranch_scc1 Llk_%=\n" \
  "  s_mov_b32 s64, 0x2000\n" \
  "  s_branch Lpk_%=\n" \
  "LKs_%=:\n" \
  "  s_waitcnt lgkmcnt(0)\n" \
  "  s_add_u32 s62, s68, 25\n" \
  "  s_cmp_eq_u32 s40, 0\n" \
  "  s_cbranch_scc1 LKt_%=\n" \
  "  s_mov_b32 s16, 0\n" \
  "LKs2_%=:\n" \
  "  buffer_load_dwordx2 v[174:175], off, s[36:39], s66 sc1\n" \
  "  s_waitcnt vmcnt(0)\n" \
  "  v_readfirstlane_b32 s62, v174\n" \
  "  v_readfirstlane_b32 s67, v175\n" \
  "  s_nop 0\n" \
  "  s_cmp_eq_u32 s67, s40\n" \
  "  s_cbranch_scc1 LKt_%=\n" \
  "  s_add_u32 s16, s16, 1\n" \
  "  s_cmp_lt_u32 s16, 3\n" \
  "  s_cbranch_scc1 LKs2_%=\n" \
  "  s_branch LKu_%=\n" \
  "LKe_%=:\n" \
  "  s_barrier\n" \
  "  s_cmp_eq_u32 s31, 0\n" \
  "  s_cbranch_scc1 LKoe_%=\n" \
  "  v_mov_b32_e32 v172, s40\n" \
  "  v_add_u32_e32 v172, -1, v172\n" \
  "  s_mov_b64 exec, 1\n" \
  "  buffer_store_dword v172, off, s[36:39], s31 sc1\n" \
  "  s_mov_b64 exec, -1\n" \
  "LKoe_%=:\n" \
  "  s_endpgm\n" \
  ".Lfkk1:\n"

#define CHAIN_R_ASM \
  "  s_mov_b32 s40, 0\n" \
  "  s_mov_b32 s41, 0\n" \
  "  s_mov_b32 s42, 0\n" \
  "  s_mov_b32 s64, 0\n" \
  "  s_memrealtime s[66:67]\n" \
  "  s_waitcnt lgkmcnt(0)\n" \
  "  s_mov_b32 s28, s66\n" \
  "  v_mov_b64_e32 v[52:53], -1\n" \
  "  v_mov_b64_e32 v[54:55], -1\n" \
  "LR_%=:\n" \
  "  s_add_u32 s53, s42, 32768\n" \
  "  s_and_b32 s53, s53, 131071\n" \
  "  s_add_u32 s27, s53, 16384\n" \
  "  s_add_u32 s54, s46, s41\n" \
  "  s_add_u32 s55, s54, 1024\n" \
  "  s_add_u32 s56, s54, 2048\n" \
  "  s_add_u32 s57, s54, 3072\n" \
  "  s_add_u32 s58, s54, 32768\n" \
  "  s_add_u32 s59, s54, 33792\n" \
  "  s_add_u32 s60, s54, 34816\n" \
  "  s_add_u32 s61, s54, 35840\n" \
  "  s_mov_b32 s65, 0\n" \
  "  s_bfe_u32 s47, s40, 0x10001\n" \
  "  s_memrealtime s[68:69]\n" \
  "  s_waitcnt lgkmcnt(0)\n" \
  "Ldp_%=:\n" \
  "  s_sub_u32 s62, s68, s28\n" \
  "  s_memrealtime s[68:69]\n" \
  "  s_add_u32 s62, s62, 2000\n" \
  "  s_cmp_lt_u32 s62, 1997\n" \
  "  s_cbranch_scc1 Ldp_%=\n" \
  "  s_cmp_lt_u32 s62, 2000\n" \
  "  s_cbranch_scc0 Lep_%=\n" \
  "Lfp_%=:\n" \
  "  s_add_u32 s62, s62, 1\n" \
  "  s_cmp_lt_u32 s62, 2000\n" \
  "  s_cbranch_scc1 Lfp_%=\n" \
  "Lep_%=:\n" \
  "Llp_%=:\n" \
  "  buffer_load_dwordx4 v[16:19], v0, s[36:39], s54 offen sc1\n" \
  "  buffer_load_dwordx4 v[20:23], v0, s[36:39], s55 offen sc1\n" \
  "  buffer_load_dwordx4 v[24:27], v0, s[36:39], s56 offen sc1\n" \
  "  buffer_load_dwordx4 v[28:31], v0, s[36:39], s57 offen sc1\n" \
  "  buffer_load_dwordx4 v[32:35], v0, s[36:39], s58 offen sc1\n" \
  "  buffer_load_dwordx4 v[36:39], v0, s[36:39], s59 offen sc1\n" \
  "  buffer_load_dwordx4 v[40:43], v0, s[36:39], s60 offen sc1\n" \
  "  buffer_load_dwordx4 v[44:47], v0, s[36:39], s61 offen sc1\n" \
  "  s_waitcnt vmcnt(7)\n" \
  "  v_mfma_f32_16x16x32_bf16 v[8:11], v[16:19], v[4:7], 0\n" \
  "  v_bitop3_b32 v49, v16, v18, s47 bitop3:0x7e\n" \
  "  s_waitcnt vmcnt(6)\n" \
  "  v_mfma_f32_16x16x32_bf16 v[8:11], v[20:23], v[4:7], v[8:11]\n" \
  "  v_bitop3_b32 v50, v20, v22, s47 bitop3:0x7e\n" \
  "  v_or_b32_e32 v48, v49, v50\n" \
  "  s_waitcnt vmcnt(5)\n" \
  "  v_mfma_f32_16x16x32_bf16 v[8:11], v[24:27], v[4:7], v[8:11]\n" \
  "  v_bitop3_b32 v49, v24, v26, s47 bitop3:0x7e\n" \
  "  s_waitcnt vmcnt(4)\n" \
  "  v_mfma_f32_16x16x32_bf16 v[8:11], v[28:31], v[4:7], v[8:11]\n" \
  "  v_bitop3_b32 v50, v28, v30, s47 bitop3:0x7e\n" \
  "  v_or3_b32 v48, v48, v49, v50\n" \
  "  s_waitcnt vmcnt(3)\n" \
  "  v_mfma_f32_16x16x32_bf16 v[8:11], v[32:35], v[4:7], v[8:11]\n" \
  "  v_bitop3_b32 v49, v32, v34, s47 bitop3:0x7e\n" \
  "  s_waitcnt vmcnt(2)\n" \
  "  v_mfma_f32_16x16x32_bf16 v[8:11], v[36:39], v[4:7], v[8:11]\n" \
  "  v_bitop3_b32 v50, v36, v38, s47 bitop3:0x7e\n" \
  "  v_or3_b32 v48, v48, v49, v50\n" \
  "  s_waitcnt vmcnt(1)\n" \
  "  v_mfma_f32_16x16x32_bf16 v[8:11], v[40:43], v[4:7], v[8:11]\n" \
  "  v_bitop3_b32 v49, v40, v42, s47 bitop3:0x7e\n" \
  "  s_waitcnt vmcnt(0)\n" \
  "  v_mfma_f32_16x16x32_bf16 v[8:11], v[44:47], v[4:7], v[8:11]\n" \
  "  v_bitop3_b32 v50, v44, v46, s47 bitop3:0x7e\n" \
  "  v_or3_b32 v48, v48, v49, v50\n" \
  "  v_and_b32_e32 v48, 1, v48\n" \
  "  v_cmp_ne_u32_e32 vcc, 0, v48\n" \
  "  s_nop 3\n" \
  "  v_permlane32_swap_b32_e32 v8, v10\n" \
  "  v_permlane32_swap_b32_e32 v9, v11\n" \
  "  v_add_f32_e32 v8, v8, v10\n" \
  "  v_add_f32_e32 v9, v9, v11\n" \
  "  s_cbranch_vccnz Lnp_%=\n" \
  "Lpp_%=:\n" \
  "  v_cvt_pk_bf16_f32 v12, v8, v9\n" \
  "  v_min_u32_e32 v12, -2, v12\n" \
  "  v_mov_b32_e32 v13, v12\n" \
  "  s_nop 1\n" \
  "  v_permlane32_swap_b32_e32 v12, v13\n" \
  "  buffer_store_dwordx2 v[12:13], v1, s[36:39], s53 offen sc1\n" \
  "  buffer_store_dwordx2 v[12:13], v1, s[36:39], s27 offen sc1\n" \
  "  buffer_store_dwordx2 v[52:53], v1, s[36:39], s42 offen sc1\n" \
  "  s_add_u32 s27, s42, 16384\n" \
  "  buffer_store_dwordx2 v[52:53], v1, s[36:39], s27 offen sc1\n" \
  "  s_mov_b32 s42, s53\n" \
  "  s_sub_u32 s41, 65536, s41\n" \
  "  s_add_u32 s28, s28, s29\n" \
  "  s_add_u32 s40, s40, 1\n" \
  "  s_cmp_lt_u32 s40, 63\n" \
  "  s_cbranch_scc0 LRe_%=\n" \
  "  s_memrealtime s[68:69]\n" \
  "  s_waitcnt lgkmcnt(0)\n" \
  "Ldw_%=:\n" \
  "  s_sub_u32 s62, s68, s28\n" \
  "  s_memrealtime s[68:69]\n" \
  "  s_add_u32 s62, s62, 2000\n" \
  "  s_cmp_lt_u32 s62, 1997\n" \
  "  s_cbranch_scc1 Ldw_%=\n" \
  "  s_cmp_lt_u32 s62, 2000\n" \
  "  s_cbranch_scc0 Lew_%=\n" \
  "Lfw_%=:\n" \
  "  s_add_u32 s62, s62, 1\n" \
  "  s_cmp_lt_u32 s62, 2000\n" \
  "  s_cbranch_scc1 Lfw_%=\n" \
  "Lew_%=:\n" \
  "  s_and_b32 s62, s40, 3\n" \
  "  s_lshl_b32 s62, s62, 15\n" \
  "  s_add_u32 s62, s62, s51\n" \
  "  s_mov_b32 s63, s64\n" \
  "LRw_%=:\n" \
  "  buffer_load_dwordx2 v[72:73], off, s[36:39], s62 sc1\n" \
  "  s_waitcnt vmcnt(0)\n" \
  "  v_readfirstlane_b32 s68, v72\n" \
  "  v_readfirstlane_b32 s69, v73\n" \
  "  s_add_u32 s63, s63, 1\n" \
  "  s_cmp_eq_u32 s69, s40\n" \
  "  s_cselect_b32 s28, s68, s28\n" \
  "  s_cselect_b32 s63, 0x2000, s63\n" \
  "  s_cmp_lt_u32 s63, 0x2000\n" \
  "  s_cbranch_scc1 LRw_%=\n" \
  "  s_branch LR_%=\n" \
  "Lnp_%=:\n" \
  "  s_add_u32 s65, s65, 1\n" \
  "  s_max_u32 s65, s65, s64\n" \
  "  s_cmp_lt_u32 s65, 0x2000\n" \
  "  s_cbranch_scc1 Llp_%=\n" \
  "  s_mov_b32 s64, 0x2000\n" \
  "  s_branch Lpp_%=\n" \
  "LRe_%=:\n" \
  "  s_endpgm\n"
#define CHAIN_Q_CLOB "v196", "v197", "v198", "v199", "v200", "v201", "v202", "v203", "v204", "v205", "v206", "v207", "v208", "v209", "v210", "v211", "v212", "v213", "v214", "v215", "v216", "v217", "v218", "v219", "v220", "v221", "v222", "v223", "v224", "v225", "v226", "v227", "v228", "v229", "v230", "v231", "v232", "v233", "v234", "v235", "v236", "v237", "v238", "v239", "v240", "v241", "v242", "v243", "v244", "v245", "v246", "v247", "v248", "v249", "v250", "v251", "v252", "v253", "v254", "v255", "s22", "s23", "s24", "s25", "s26", "s29", "s31", "s34", "s35", "s40", "s41", "s42", "s43", "s44", "s48", "s49", "s50", "s51", "s52", "s53", "s54", "s55", "s56", "s57", "s58", "s59", "s60", "s61", "s62", "s63", "s64", "s65", "s66", "s67", "s68", "s69", "s71", "s72", "s73", "s74", "s75", "s76", "s77", "s78", "s79", "s80", "s81", "s82", "s83", "s84", "s85", "s86", "s87", "s88", "s89", "s90", "s91", "s92", "s93", "s94", "s95", "s96", "s97", "s98", "s99", "s100", "s101", "vcc", "scc", "memory"
#define CHAIN_K_CLOB "v176", "v177", "v178", "v179", "v180", "v181", "v182", "v183", "v184", "v185", "v186", "v187", "v188", "v189", "v190", "v191", "v192", "v193", "v194", "v195", "v196", "v197", "v198", "v199", "v200", "v201", "v202", "v203", "v204", "v205", "v206", "v207", "v208", "v209", "v210", "v211", "v212", "v213", "v214", "v215", "v216", "v217", "v218", "v219", "v220", "v221", "v222", "v223", "v224", "v225", "v226", "v227", "v228", "v229", "v230", "v231", "v232", "v233", "v234", "v235", "v236", "v237", "v238", "v239", "v240", "v241", "v242", "v243", "v244", "v245", "v246", "v247", "v248", "v249", "v250", "v251", "v252", "v253", "v254", "v255", "s14", "s15", "s18", "s19", "s23", "s24", "s27", "s28", "s30", "s34", "s35", "s40", "s41", "s42", "s45", "s47", "s49", "s52", "s53", "s54", "s55", "s56", "s57", "s58", "s59", "s60", "s61", "s62", "s64", "s65", "s67", "s68", "s69", "s71", "s72", "s73", "s74", "s75", "s76", "s77", "s78", "s79", "s80", "s81", "s82", "s83", "s84", "s85", "s86", "s87", "s88", "s89", "s90", "s91", "s92", "s93", "s94", "s95", "s96", "s97", "s98", "s99", "s100", "s101", "vcc", "scc", "memory"
#define CHAIN_R_CLOB "v2", "v3", "v8", "v9", "v10", "v11", "v12", "v13", "v14", "v15", "v16", "v17", "v18", "v19", "v20", "v21", "v22", "v23", "v24", "v25", "v26", "v27", "v28", "v29", "v30", "v31", "v32", "v33", "v34", "v35", "v36", "v37", "v38", "v39", "v40", "v41", "v42", "v43", "v44", "v45", "v46", "v47", "v48", "v49", "v50", "v51", "v52", "v53", "v54", "v55", "v56", "v57", "v58", "v59", "v60", "v61", "v62", "v63", "v64", "v65", "v66", "v67", "v68", "v69", "v70", "v71", "v72", "v73", "v74", "v75", "s27", "s28", "s40", "s41", "s42", "s43", "s44", "s45", "s47", "s48", "s49", "s50", "s52", "s53", "s54", "s55", "s56", "s57", "s58", "s59", "s60", "s61", "s62", "s63", "s64", "s65", "s66", "s67", "s68", "s69", "s71", "s72", "s73", "s74", "s75", "s76", "s77", "s78", "s79", "s80", "s81", "s82", "s83", "s84", "s85", "s86", "s87", "s88", "s89", "s90", "s91", "s92", "s93", "s94", "s95", "s96", "s97", "s98", "s99", "s100", "s101", "vcc", "scc", "memory"

#define W_OPS "{v[0:15]}"(cat88(W[0][0], W[1][0])), "{v[16:31]}"(cat88(W[0][1], W[1][1])), "{v[32:47]}"(cat88(W[0][2], W[1][2])), "{v[48:63]}"(cat88(W[0][3], W[1][3])), "{v[64:79]}"(cat88(W[0][4], W[1][4])), \
  "{v[80:95]}"(cat88(W[0][5], W[1][5])), "{v[96:111]}"(cat88(W[0][6], W[1][6])), "{v[112:127]}"(cat88(W[0][7], W[1][7])), "{v[128:143]}"(cat88(W[0][8], W[1][8]))
DEVI unsigned sfl(unsigned x) { return __builtin_amdgcn_readfirstlane(x); }

typedef const __attribute__((address_space(4))) Params* kparg;
DEVI Params ldp(kparg k) { Params p; p.x = k->x; p.mem0 = k->mem0; p.Wq = k->Wq; p.Wk = k->Wk; p.Wv = k->Wv; p.Wo = k->Wo; p.Wg = k->Wg; p.Wu = k->Wu; p.Wd = k->Wd; p.out = k->out; p.ws = k->ws; p.t0 = k->t0; p.t1 = k->t1; p.t2 = k->t2; p.t3 = k->t3; return p; }
#define ROLE_ENTER kparg kp_ = (kparg)__builtin_amdgcn_kernarg_segment_ptr(); int b = b0, w = w0; asm volatile("" : "+s"(kp_), "+s"(b), "+s"(w)); const Params p = ldp(kp_); \
  const unsigned long wsa = (unsigned long)p.ws; const v4u rs = {sfl((unsigned)wsa), sfl((unsigned)(wsa >> 32) & 0xffffu), WS_TOTAL, 0x00020000u}; \
  const int h = 4 * (b & 7) + ((b >> 4) & 3), hf = (b >> 3) & 1, w3 = w & 3; unsigned dg = sfl(WS_DIAG + (b * 8 + w) * 64); (void)h; (void)hf; (void)w3
DEVI v2u bl64(rsrc_t r, unsigned vo, unsigned so) { return __builtin_amdgcn_raw_buffer_load_b64(r, vo, so, 16); }
DEVI void hs_master(unsigned char* ws, unsigned nl, unsigned nh) {
  const int lane = LANE_ID("ms");
  const rsrc_t r = __builtin_amdgcn_make_buffer_rsrc((void*)ws, 0, WS_TOTAL, 0x00020000);
  ldsw32(lane * 8, ~0u); ldsw32(lane * 8 + 4, ~0u); ldsw32(512 + lane * 4, 0u);
  v4u e[4];
  for (unsigned k = 0; k < 0x10000u; ++k) {
    asm volatile("" ::: "memory");
    bool ok = true;
#pragma unroll
    for (int i = 0; i < 4; ++i) { e[i] = bl128(r, lane * 16, H_R + i * 1024); }
#pragma unroll
    for (int i = 0; i < 4; ++i) ok = ok & (e[i].x == nl) & (e[i].z >> 16 == nh);
    if (__all(ok)) break;
  }
#pragma unroll
  for (int i = 0; i < 4; ++i) __hip_atomic_fetch_min(LP(unsigned, (e[i].w >> 28) * 32 + (e[i].w & 7) * 4), e[i].w & 0xfffffffu, __ATOMIC_RELAXED, __HIP_MEMORY_SCOPE_WORKGROUP);
  unsigned j[4], rg[2] = {~0u, lane < 8 ? ~0u : 0u};
#pragma unroll
  for (int i = 0; i < 4; ++i) {
    const v4u a = ldsr128((e[i].w >> 28) * 32), c = ldsr128((e[i].w >> 28) * 32 + 16);
    j[i] = min(min(min(a.x, a.y), min(a.z, a.w)), min(min(c.x, c.y), min(c.z, c.w))) & 7u;
  }
#pragma unroll 1
  for (unsigned k = 0; k < 8 && __any(max(rg[0], rg[1]) == ~0u); ++k) {
#pragma unroll
    for (int i = 0; i < 2; ++i)
      if (rg[i] == ~0u) {
        const unsigned c = j[i] ^ (k & 1u) ^ ((k & 2u) << 1) ^ ((k & 4u) >> 1);
        const unsigned t = __hip_atomic_fetch_add(LP(unsigned, 512 + c * 4), 1u, __ATOMIC_RELAXED, __HIP_MEMORY_SCOPE_WORKGROUP);
        if (t < 9u) rg[i] = t * 8u + c;
      }
  }
#pragma unroll
  for (int i = 0; i < 4; ++i) {
    const v2u v = {e[i].y, (e[i].z & 0xffffu) | (j[i] ^ 1u) << 16 | (i < 2 ? min(rg[i < 2 ? i : 0], 71u) : 0u) << 24};
    __builtin_amdgcn_raw_buffer_store_b64(v, r, lane * 8, H_G + i * 512, 16);
  }
}
DEVI void hs_reply(rsrc_t r, int b, unsigned nl, unsigned nh, unsigned smp, v2u c) {
  const v4u v = {c.x, nl, nh | c.y << 16, smp};
  if (LANE_ID("hr") == 0) { bs128(v, r, 0, H_R + b * 16); ldsw128(L_HS + 16, v); }
}
DEVI void hs_start(const Params& p, int b, int w) {
  unsigned char* ws = p.ws;
  const rsrc_t r = __builtin_amdgcn_make_buffer_rsrc((void*)ws, 0, WS_TOTAL, 0x00020000);
  const unsigned long n = __builtin_amdgcn_s_memrealtime();
  const unsigned nl = (unsigned)n, nh = (unsigned)(n >> 32) & 0xffffu;
  unsigned smp = 0;
  v2u ch = {};
  if (b == 64 && w == 1) { const v2u v = {nl, nh}; if (LANE_ID("hm") == 0) __builtin_amdgcn_raw_buffer_store_b64(v, r, 0, H_CH, 16); }
  if (w == 0) {
    const unsigned c = (b >> 3) & 7, so = part_base(c) + PART_SLOT + ((b >> 6) * 8 + (b & 7)) * 128;
    const unsigned g0 = __builtin_amdgcn_raw_buffer_load_b32(r, 0, so ^ 128u, 16);
    asm volatile("s_waitcnt vmcnt(0)" :: "v"(g0) : "memory");
    const unsigned t0 = clk();
    asm volatile("s_waitcnt lgkmcnt(0)" ::: "memory");
    const unsigned g = __builtin_amdgcn_raw_buffer_load_b32(r, 0, so, 16);
    SCHED0();
    ch = bl64(r, 0, H_CH);
    ldsw32(L_HS, 0u);
    asm volatile("s_waitcnt vmcnt(1)" :: "v"(g) : "memory");
    smp = min(clk() - t0, 0xfffffu) * 8 + c | xcc_id() << 28;
    asm volatile("s_waitcnt lgkmcnt(0)" ::: "memory");
  }
  BARN();
  if ((b >= 64 && b < 72) || b >= 216) {
    const unsigned a = b < 72 ? b - 64 : b - 208, t = threadIdx.x;
    const v4u o = {~0u, ~0u, ~0u, ~0u};
#pragma unroll
    for (int k = 0; k < 4; ++k) bs128(o, r, t * 16, WS_PART + (a * 4 + k) * 8192);
    if (a < 32 && t < 16) bs32(~0u, r, t * FS, WS_FLG + a * 16 * FS);
    asm volatile("s_waitcnt vmcnt(0)" ::: "memory");
    BARN();
  }
  if (w == 0) {
#pragma unroll 1
    for (int k = 0; k < 8 && sfl(ch.x) - nl + 0x800u > 0x1000u; ++k) { asm volatile("" ::: "memory"); ch = bl64(r, 0, H_CH); }
    hs_reply(r, b, nl, nh, smp, ch);
  }
  if (b == 64 && w == 1) hs_master(ws, nl, nh);
}
DEVI unsigned hs_wait(rsrc_t r, int b, v2u g) {
  v4u h = ldsr128(L_HS + 16);
#pragma unroll 1
  for (unsigned k = 0; k < 0x10000u && sfl(g.x != h.y || ((g.y ^ h.z) & 0xffffu) != 0); ++k) {
    asm volatile("" ::: "memory");
    const v2u c = bl64(r, 0, H_CH);
    g = bl64(r, 0, H_G + b * 8);
    if (sfl(c.x != h.x || (c.y & 0xffffu) != h.z >> 16)) { hs_reply(r, b, h.y, h.z & 0xffffu, h.w, c); h.x = c.x; h.z = (h.z & 0xffffu) | c.y << 16; }
  }
  __builtin_amdgcn_fence(__ATOMIC_ACQUIRE, "wavefront");
  asm volatile("" ::: "memory");
  return sfl(g.y) >> 16;
}
DEVI unsigned hs_wait(rsrc_t r, int b) { return hs_wait(r, b, bl64(r, 0, H_G + b * 8)); }
#define LA 4
template <bool KW>
DEVI unsigned load_weights(const Params& p, int b, int h, int hf, int w, int lane, v8u (&W)[2][9], v8u (&WO)[4]) {
  const int g = lane >> 4, q4 = (lane >> 2) & 3, p4 = lane & 3, w3 = w & 3;
  const rsrc_t rs = __builtin_amdgcn_make_buffer_rsrc((void*)p.ws, 0, WS_TOTAL, 0x00020000);
  const u16* src = (KW ? p.Wk : p.Wq) + h * 128 + w3 * 32 + (size_t)(lane >> 2) * HD_ + p4 * 8;
  const u16* so = p.Wo + (size_t)(h * 128 + hf * 64) * D_;
  const lptr st = w < 2 ? L_KX + XB + w * 4096 : w < 4 ? L_QX + XB + (w - 2) * 4096 : L_VX + VB + (w - 4) * 4096;
  const int mc = min(w3 * 64 + lane + (KW ? 256 : 0), 287);
  LST(0);
  unsigned cw = 0;
  if (!KW && !T2(64)) {
    unsigned lo, hi, d, n;
    if (w == 0) asm volatile("s_getpc_b64 s[34:35]\n.Lfkh0:\n\ts_mov_b32 %0, s34\n\ts_mov_b32 %1, s35\n\ts_mov_b32 %2, .Lfkq0-.Lfkh0-1024\n\ts_mov_b32 %3, .Lfkq1-.Lfkq0+1020" : "=s"(lo), "=s"(hi), "=s"(d), "=s"(n) :: "s34", "s35");
    else asm volatile("s_getpc_b64 s[34:35]\n.Lfkh1:\n\ts_mov_b32 %0, s34\n\ts_mov_b32 %1, s35\n\ts_mov_b32 %2, .Lfkk0-.Lfkh1-1024\n\ts_mov_b32 %3, .Lfkk1-.Lfkk0+1020" : "=s"(lo), "=s"(hi), "=s"(d), "=s"(n) :: "s34", "s35");
    const char* a = (const char*)(((unsigned long)hi << 32 | lo) + (long)(int)d) + min((unsigned)(lane * 64 + max(w - 1, 0) * 4096), n);
    asm volatile("global_load_dword %0, %1, off" : "=v"(cw) : "v"(a) : "memory");
  }
  constexpr int NV = KW ? 2 : 3;
  const int v0 = KW ? 10 + 2 * w3 : (5 * w) >> 1;
#pragma unroll
  for (int i = 0; i < 4 * NV; ++i) {
    const int row = (v0 * 4 + i) * 8 + (lane >> 3), ch = lane & 7, gr = (ch >> 1) ^ (((row >> 1) & 1) | (((row >> 3) & 1) << 1));
    if (i < 8 || (w & 1)) dma16(p.Wv + h * 128 + hf * 64 + (size_t)row * HD_ + gr * 16 + (ch & 1) * 8, sfl(L_WV + (v0 * 4 + i) * 1024));
  }
  v4u raw[12][4], m0[2];
  auto issue = [&](int k) {
#pragma unroll
    for (int i = 0; i < 4; ++i) {
      if (k < 9) raw[k][i] = gl128<2>(src + (size_t)(k * 4 + i) * 16 * HD_);
      else if (k < 11) raw[k][i] = gl128<2>(so + (size_t)(((k - 9) * 4 + i) * 8 + (lane >> 3)) * D_ + w * 64 + (lane & 7) * 8);
      else if (KW) raw[k][i] = gl128<2>(so + (size_t)(i * 16 + (lane >> 2)) * D_ + 512 + (w3 & 1) * 32 + p4 * 8);
    }
    if (k == 11) { m0[0] = gl128<0>(p.mem0 + (mc & 7) * D_ + 16 * (mc >> 3)); m0[1] = gl128<0>(p.mem0 + (mc & 7) * D_ + 16 * (mc >> 3) + 8); }
  };
#pragma unroll
  for (int k = 0; k < LA; ++k) issue(k);
#pragma unroll
  for (int k = 0; k < 12; ++k) {
    SCHED0();
    if (k + LA < 12) issue(k + LA);
    SCHED0();
    if (k < 9) {
#pragma unroll
      for (int i = 0; i < 4; ++i) ldsw128(st + i * 1024 + lane * 16, raw[k][i]);
      const lptr a = st + (8 * g + q4) * 64 + p4 * 8;
#pragma unroll
      for (int t = 0; t < 2; ++t) W[t][k] = cat8(ldstr(a + t * 32), ldstr(a + t * 32 + 256), ldstr(a + t * 32 + 2048), ldstr(a + t * 32 + 2048 + 256));
      if (k % 3 == 2) LST(1 + k / 3);
      if (k < NV && (k < 2 || (w & 1))) {
        asm volatile("" ::: "memory");
        const lptr a = L_WV + (v0 + k) * 4096 + (8 * g + q4) * 128 + 8 * p4;
        v4u f[4];
#pragma unroll
        for (int i = 0; i < 4; ++i) { const lptr c = a + 32 * (i ^ ((q4 >> 1) | ((g & 1) << 1))); f[i] = cat4(ldstr(c), ldstr(c + 512)); }
        asm volatile("s_waitcnt lgkmcnt(0)" : "+v"(f[0]), "+v"(f[1]), "+v"(f[2]), "+v"(f[3]) :: "memory");
#pragma unroll
        for (int i = 0; i < 4; ++i) ldsw128(L_WV + (v0 + k) * 4096 + i * 1024 + lane * 16, f[i]);
      }
    } else if (k < 11) {
#pragma unroll
      for (int i = 0; i < 4; ++i) { const v4u r = raw[k][i], s = {r.x, r.z, r.y, r.w}; ldsw128(st + i * 1024 + lane * 16, s); }
      const lptr a = st + (4 * g + (q4 & 1)) * 128 + (2 * (q4 >> 1) + (p4 >> 1)) * 32 + (p4 & 1) * 8;
#pragma unroll
      for (int t = 0; t < 2; ++t) WO[t * 2 + k - 9] = cat8(ldstr(a + t * 16), ldstr(a + t * 16 + 256), ldstr(a + t * 16 + 2048), ldstr(a + t * 16 + 2048 + 256));
    } else if (KW) {
#pragma unroll
      for (int i = 0; i < 4; ++i) { const v4u r = raw[k][i], s = {r.x, r.z, r.y, r.w}; ldsw128(st + i * 1024 + lane * 16, s); }
      const lptr a = st + (4 * g + (q4 & 1)) * 64 + (2 * (q4 >> 1) + (p4 & 1)) * 16 + (p4 >> 1) * 8;
      const v8u e0 = cat8(ldstr(a), ldstr(a + 128), ldstr(a + 1024), ldstr(a + 1024 + 128)), e1 = cat8(ldstr(a + 2048), ldstr(a + 2048 + 128), ldstr(a + 3072), ldstr(a + 3072 + 128));
      lptr o = L_WO + w3 * 4096 + lane * 16;
      ldsw128(o, lo4(WO[2])); ldsw128(o + 1024, hi4(WO[2])); ldsw128(o + 2048, lo4(WO[3])); ldsw128(o + 3072, hi4(WO[3]));
      if (w3 < 2) { o += 16384; ldsw128(o, lo4(e0)); ldsw128(o + 1024, hi4(e0)); ldsw128(o + 2048, lo4(e1)); ldsw128(o + 3072, hi4(e1)); }
    }
  }
  {
    const v4u v0 = {m0[0].x, m0[0].z, m0[1].x, m0[1].z}, v1 = {m0[0].y, m0[0].w, m0[1].y, m0[1].w};
    const lptr o = L_MEM + (mc >> 3) * 256 + (4 * ((mc & 7) >> 1) + (mc & 1)) * 16;
    ldsw128(o, v0); ldsw128(o + 32, v1);
    if (w == 0 && lane == 0) ldsw32(L_OC, 0);
  }
  if (KW && w3 == 3) {
    asm volatile("s_waitcnt lgkmcnt(0)" ::: "memory");
    const v4u z4 = {0u, 0u, 0u, 0u};
    ldsw128(L_SEL + lane * 16, sel_matrix(lane & 15)); ldsw128(L_FL + lane * 16, z4);
  }
  asm volatile("" :: "v"(cw));
  LST(4);
  unsigned gr = 0;
  if (w == 0) {
    gr = hs_wait(rs, b);
    int f = -1;
#pragma unroll 1
    for (unsigned k = 0; k < SPINCAP * 8 && f < 0; ++k) {
      asm volatile("" ::: "memory");
      const int v = (int)__builtin_amdgcn_raw_buffer_load_b32(rs, min(lane, 2) * FS, F_Q0 + h * 4 * FS, 16);
      f = min(min(__builtin_amdgcn_readlane(v, 0), __builtin_amdgcn_readlane(v, 1)), __builtin_amdgcn_readlane(v, 2));
    }
    gr |= (unsigned)(max(f, 0) + 1) << 16;
    if (lane == 0) ldsw32(L_HS, gr);
  } else {
#pragma unroll 1
    for (unsigned k = 0; k < (1u << 22) && (gr = sfl(*LP(volatile unsigned, L_HS))) == 0; ++k) __builtin_amdgcn_s_sleep(1);
  }
  asm volatile("" ::: "memory");
  LST(5);
  if (!KW) {
    const unsigned l16 = lane * 16, xo = h * HB + w * 2048;
    const v4u x0 = bl128(rs, l16, WS_K + xo), x1 = bl128(rs, l16 + 1024, WS_K + xo), x2 = bl128(rs, l16, WS_V + h * HB + hf * 4096 + w * 1024), x3 = bl128(rs, l16, WS_Q + xo), x4 = bl128(rs, l16 + 1024, WS_Q + xo);
    ldsw128(L_KX + w * 2048 + l16, x0); ldsw128(L_KX + w * 2048 + 1024 + l16, x1); ldsw128(L_VX + w * 1024 + l16, x2); ldsw128(L_QX + w * 2048 + l16, x3); ldsw128(L_QX + w * 2048 + 1024 + l16, x4);
  }
  LST(6);
  asm volatile("s_waitcnt lgkmcnt(0)" ::: "memory");
  return sfl(gr);
}
DEVI void pf_all(const Params& p, int b, int w, unsigned t0) {
  if (T2(32) || (b >= 120 && b < 216)) return;
  const int slot = (b >> 3) == 8 ? 0 : (b >> 3) - 14;
  const u16* a = p.Wo + (size_t)(b & 7) * 512 * D_ + (slot * 8 + w) * 2048 + LANE_ID("pf") * 8;
  while (rt() - t0 < ((unsigned)p.t1 >> 16 & 0xff) * 4) __builtin_amdgcn_s_sleep(8);
  v4u r[4];
#pragma unroll
  for (int k = 0; k < 4; ++k) r[k] = gl128<1>(a + k * 512);
#pragma unroll
  for (int k = 0; k < 4; ++k) asm volatile("" :: "v"(r[k]));
}
DEVI void chain_wg(const int b0, const int w0) {
  if (b0 >= 64) {
    ROLE_ENTER;
    pf_all(p, b, w, rt());
    if (w != 0) return;
    const int lane = LANE_ID("r");
    const unsigned gr = hs_wait(__builtin_amdgcn_make_buffer_rsrc((void*)p.ws, 0, WS_TOTAL, 0x00020000), b), j = gr & 7, rg = gr >> 8;
    asm volatile(CHAIN_R_ASM : "+{s70}"(dg) : "{v0}"(lane * 16), "{v1}"(R_OFF(rg)), "{v[4:7]}"(sel_matrix(lane & 15)), "{s[36:39]}"(rs), "{s46}"(sfl(part_base(rg))), "{s29}"(sfl(135)), "{s51}"(sfl(WS_TIME + j * 4096)) : CHAIN_R_CLOB);
    __builtin_unreachable();
  }
  if (w0 >= 4) {
    ROLE_ENTER;
    const int lane = LANE_ID("k"); LANE_VARS;
    v8u W[2][9], WO[4];
    const unsigned gr = load_weights<true>(p, b, h, hf, w, lane, W, WO);
    const unsigned t3 = sfl(p.t3 & 0xff);
    const unsigned j = gr & 7, rg = (gr >> 8) & 0xff;
    v16u m;
    m[0] = l16; m[1] = (n & 2) ? 0xEEEE : 0x4444; m[2] = ST_OFF; m[3] = R_OFF(rg);
    m[4] = l16 & 0x37f; m[5] = ST5_OFF; m[6] = L_WV + w3 * 1024 + l16; m[7] = n < 8 ? 0x4444 : 0xEEEE;
    m[8] = L_KI + (32 * w3 + n) * 32 + g * 8; m[9] = L_VM + w3 * 512 + lane * 8; m[10] = 0; m[11] = 0; m[12] = 0; m[13] = 0; m[14] = 0; m[15] = 0;
    unsigned s66 = sfl(WS_K + SEGB + h * HB + w3 * 2048), s43 = sfl(L_KX + XB + w3 * 2048), s44 = sfl(L_VX + VB + w3 * 1024), s16 = gr >> 16;
    asm volatile(CHAIN_K_ASM
      : "+{v[160:175]}"(m), "+{s70}"(dg), "+{s66}"(s66), "+{s16}"(s16), "+{s43}"(s43), "+{s44}"(s44)
      : W_OPS, "{v[144:159]}"(cat88(WO[0], WO[1])), "{s[36:39]}"(rs), "{s20}"(sfl(G_MAIN(j))), "{s21}"(sfl(G_EXTRA(j))), "{s22}"(sfl(w * 1024)), "{s46}"(sfl(part_base(rg))),
        "{s48}"(sfl(w3)), "{s29}"(t3), "{s51}"(sfl(WS_TIME + j * 4096)), "{s50}"(sfl(L_WO + w3 * 4096)), "{s25}"(sfl(L_WO + 16384 + (w3 & 1) * 4096)),
        "{s31}"(sfl(w3 == 0 ? F_O + (h * 2 + hf) * FS : 0)), "{s63}"(sfl(WS_V - WS_K + hf * 4096 - w3 * 1024)), "{s17}"(sfl(F_QKV + ((h >> 2) + 8 * ((h >> 1) & 1)) * 4 * FS))
      : CHAIN_K_CLOB);
    __builtin_unreachable();
  } else {
    ROLE_ENTER;
    const int lane = LANE_ID("q"); LANE_VARS;
    v8u W[2][9], WO[4];
    const unsigned j = load_weights<false>(p, b, h, hf, w, lane, W, WO) & 7;
    const int rtile = w & 1, et = (w >> 1) * 2;
    v16u m;
    m[0] = l16; m[1] = (n & 2) ? 0xEEEE : 0x4444; m[2] = ST_OFF; m[3] = (8 * g + q4) * 16 + (p4 & 1) * 8; m[4] = g * 512 + n * 16;
    { const int r = 4 * g + q4; m[5] = L_VX + w * 1024 + (p4 >> 1) * 512 + (r ^ (8 * (p4 >> 1))) * 16 + (p4 & 1) * 8; }
    m[6] = L_OI + (w >> 1) * 1024 + (2 * (w & 1) + (g >> 1)) * 256 + (n & 7) * 16 + (g & 1) * 8; m[7] = L_QI + (32 * w3 + n) * 16 + g * 4;
    m[8] = WS_O + h * HB + (unsigned)((8 * hf + 2 * (et + (g & 1)) + (g >> 1)) * 512 + (16 * rtile + n) * 16); m[9] = L_VM + w * 512 + lane * 8;
    m[10] = n < 8 ? 0x4444 : 0xEEEE; m[11] = (8 * g + q4) * 32 + p4 * 8; m[12] = m[13] = m[14] = m[15] = 0x3f803f80u;
    v4u e = {(unsigned)(rtile ? 99 : n - 4 * g), (unsigned)(rtile ? n - 4 * g : -1), (unsigned)n, 0u};
    unsigned s16 = sfl((w < 2 ? WS_K : WS_Q) + SEGB + h * HB + 6144 + (w & 1) * 1024), s17 = sfl(WS_V + SEGB + h * HB + hf * 4096 + 3072 + w * 256);
    unsigned s19 = sfl((w < 2 ? L_KX : L_QX) + 6144 + (w & 1) * 1024), s20 = sfl(L_VX + 3072 + w * 256);
    asm volatile(CHAIN_Q_ASM
      : "+{v[176:191]}"(m), "+{v[192:195]}"(e), "+{s70}"(dg), "+{s16}"(s16), "+{s17}"(s17), "+{s19}"(s19), "+{s20}"(s20)
      : W_OPS, "{v[144:159]}"(cat88(WO[0], WO[1])), "{v[160:175]}"(cat88(WO[2], WO[3])), "{s[36:39]}"(rs), "{s45}"(sfl(rtile * 256)), "{s46}"(sfl(rtile * 1024)), "{s47}"(sfl(rtile * 512)), "{s30}"(sfl(F_O + (h * 2 + hf) * FS)),
        "{s14}"(sfl(G_MAIN(j))), "{s15}"(sfl(G_EXTRA(j))), "{s18}"(sfl(w * 1024))
      : CHAIN_Q_CLOB);
    __builtin_unreachable();
  }
}

DEVI void clock_wg(const Params& p) {
  const int lane = LANE_ID("c");
  const rsrc_t rs = __builtin_amdgcn_make_buffer_rsrc((void*)p.ws, 0, WS_TOTAL, 0x00020000);
  const unsigned rep = WS_MEMB + (hs_wait(rs, 228) & 7) * 4096;
  const unsigned oa = (lane / 24) * 4 * WIN + (lane % 24) * 128, oc = 8 * WIN + (16 + (lane & 7)) * 128;
  const int gap = p.t3 & 0xff, up = (p.t3 >> 8) & 0xff, dn = (p.t3 >> 16) & 0xff, ff = p.t3 >> 24;
  unsigned I = rt(), budget = 1u << 16, c0 = clk(), t0 = I;
  const int tt0 = p.t0 & 0xff, amax = (p.t0 >> 16) & 0xff, cd = p.t0 >> 24;
  int adj = ((p.t0 >> 8) & 0xff) * 16;
  for (int s = 0; s < 62; ++s) {
    const unsigned so = rep + ((s + 1) & 3) * MEM_SLOT;
    while ((unsigned)(I + gap + cd - rt()) - 1u < 2000u) { }
    int shots = 0;
    for (;;) {
      asm volatile("" ::: "memory");
      const unsigned a = __builtin_amdgcn_raw_buffer_load_b32(rs, oa, so, 16), c = __builtin_amdgcn_raw_buffer_load_b32(rs, oc, so, 16);
      ++shots;
      if (!__any(max(a, c) == ~0u) || budget == 0) break;
      --budget;
    }
    const unsigned T = rt(), c1 = clk();
    const int dt = (int)(T - t0), dc = (int)(c1 - c0);
    const int cmp = (s > 0 && dt < 2000) ? (int)((float)(ff * 100) * (float)dt * __builtin_amdgcn_rcpf((float)dc)) : ff * 100 / 22;
    t0 = T; c0 = c1;
    if (s > 0) adj = shots > 1 ? min(adj + up * 16, amax * 16) : max(adj - dn, 0);
    const unsigned n0 = I + tt0 + cmp + (adj >> 4), n1 = T - cd + (s == 0 ? (p.t1 >> 8) & 0xff : 0) + (p.t1 & 0xff) + cmp + (adj >> 4);
    I = (int)(n0 - n1) > 0 && shots == 1 ? n0 : n1;
    const v2u v = {I, (unsigned)(s + 1)};
    if (lane < 8) {
      __builtin_amdgcn_raw_buffer_store_b64(v, rs, lane * 4096, WS_TIME + ((s + 1) & 3) * MEM_SLOT, 16);
      __builtin_amdgcn_raw_buffer_store_b64(v, rs, lane * 4096, WS_TIME + ((s + 3) & 3) * MEM_SLOT, 16);
    }
  }
}

typedef float f32x16 __attribute__((ext_vector_type(16)));
DEVI f32x16 mf3216(v4u a, v4u b, f32x16 c) { return __builtin_amdgcn_mfma_f32_32x32x16_bf16(__builtin_bit_cast(bf16x8, a), __builtin_bit_cast(bf16x8, b), c, 0, 0, 0); }
DEVI rsrc_t mkrs(const void* p, unsigned bytes) { return __builtin_amdgcn_make_buffer_rsrc((void*)p, 0, bytes, 0x00020000); }
DEVI unsigned bl32(rsrc_t r, unsigned vo, unsigned so) { return __builtin_amdgcn_raw_buffer_load_b32(r, vo, so, 16); }
#define VM0() asm volatile("s_waitcnt vmcnt(0)" ::: "memory")
DEVI int wait_words(rsrc_t r, unsigned off, int cnt, int t, int lane, unsigned& budget) {
  const unsigned vo = (lane < cnt ? lane : 0) * FS;
  for (;;) {
    asm volatile("" ::: "memory");
    const int v = (int)bl32(r, vo, off);
    if (__all(v >= t) || budget == 0) {
      int m = v;
      for (int o = 32; o; o >>= 1) m = min(m, __shfl_xor(m, o));
      __builtin_amdgcn_fence(__ATOMIC_ACQUIRE, "wavefront");
      return max(__builtin_amdgcn_readfirstlane(m), t);
    }
    --budget;
    __builtin_amdgcn_s_sleep(4);
  }
}
DEVI float gelu_mul(float g, float u) {
  const float z = g * (1.5957691216057308f + 0.07135481627260025f * g * g) * 1.4426950408889634f;
  return g * u * __builtin_amdgcn_rcpf(1.f + __builtin_amdgcn_exp2f(-z));
}

DEVI float bflo(unsigned u) { return uf(u << 16); }
DEVI float bfhi(unsigned u) { return uf(u & 0xffff0000u); }
DEVI float sum8(float a) { for (int o = 8; o < 64; o <<= 1) a += __shfl_xor(a, o); return a; }
DEVI void ln_load(const Params& p, int row0, int lane, v4u (&v)[9]) {
  const u16* src = p.x + (size_t)(row0 + (lane & 7)) * D_ + (lane >> 3) * 8;
#pragma unroll
  for (int i = 0; i < 9; ++i) gla(v[i], src + 64 * i);
}
#define LN_WAIT(n, v) asm volatile("s_waitcnt vmcnt(%9)" : "+v"(v[0]), "+v"(v[1]), "+v"(v[2]), "+v"(v[3]), "+v"(v[4]), "+v"(v[5]), "+v"(v[6]), "+v"(v[7]), "+v"(v[8]) : "n"(n) : "memory")
DEVI void ln_norm(v4u (&v)[9]) {
  float s0 = 0, s1 = 0;
  unsigned o2 = 0x3f803f80u; OPAQUE(o2);
  const bf16x2 one = __builtin_bit_cast(bf16x2, o2);
#pragma unroll
  for (int i = 0; i < 9; ++i) for (int k = 0; k < 4; k += 2) {
    s0 = __builtin_amdgcn_fdot2_f32_bf16(__builtin_bit_cast(bf16x2, (unsigned)v[i][k]), one, s0, false); s1 = __builtin_amdgcn_fdot2_f32_bf16(__builtin_bit_cast(bf16x2, (unsigned)v[i][k + 1]), one, s1, false); }
  const float mean = sum8(s0 + s1) * (1.f / D_);
  f32x2 d[36], q = {0.f, 0.f};
#pragma unroll
  for (int i = 0; i < 36; ++i) { const f32x2 x = {bflo(v[i >> 2][i & 3]), bfhi(v[i >> 2][i & 3])}; d[i] = x - mean; q = __builtin_elementwise_fma(d[i], d[i], q); }
  const float rstd = __builtin_amdgcn_rsqf(sum8(q.x + q.y) * (1.f / D_) + 1e-5f);
#pragma unroll
  for (int i = 0; i < 36; ++i) { const f32x2 y = d[i] * rstd; v[i >> 2][i & 3] = pk(y.x, y.y); }
}
DEVI void ln_seg(const Params& p, int b, int w, unsigned t0) {
  const int lane = LANE_ID("ln"), k = b - 216 - (b > 228), seg = 2 * k + (w >> 2);
  const rsrc_t rs = mkrs(p.ws, WS_TOTAL);
  if (k < 32) {
    while (rt() - t0 < ((unsigned)p.t1 >> 24) * 4) __builtin_amdgcn_s_sleep(8);
    v4u v[9];
    ln_load(p, k * 64 + w * 8, lane, v);
    LN_WAIT(0, v);
    ln_norm(v);
#pragma unroll
    for (int i = 0; i < 9; ++i) bs128(v[i], rs, (lane >> 3) * 512 + ((w & 3) * 8 + (lane & 7)) * 16, WS_XN + seg * XSEG + i * 4096);
  }
  pf_all(p, b, w, t0);
  if (w == 0) hs_wait(rs, b);
  VM0();
  BARN();
  if (k < 32 && w == 0 && lane == 0) bs32(0, rs, k * FS, F_LN);
}

DEVI void e0_wg(const Params& p, const int b, const int w) {
  const int lane = LANE_ID("e0");
  const rsrc_t rs = mkrs(p.ws, WS_TOTAL);
  const unsigned t0 = rt();
  const int c = (b - 120) >> 3, mat = c >> 2, h = 4 * (b & 7) + (c & 3);
  const u16 *wq = p.Wq, *wk = p.Wk, *wv = p.Wv; asm volatile("" : "+s"(wq), "+s"(wk), "+s"(wv));
  const u16* sa = (mat == 0 ? wq : mat == 1 ? wk : wv) + h * 128;
  v4u W[36], x0[9], pf[4];
  ln_load(p, w * 8, lane, x0);
  const int up = (lane & 31) >> 1, r1 = lane >> 5;
  const int u = (up & 7) ^ (2 * (2 * (w & 1) + r1)) ^ (up >> 3);
  const u16* src = sa + (size_t)((up >> 3) * 288 + 2 * w + r1) * HD_ + u * 16 + (lane & 1) * 8;
#pragma unroll
  for (int k = 0; k < 18; ++k) dma16(src + (size_t)k * 16 * HD_, (k * 8 + w) * 1024);
  LN_WAIT(18, x0); ln_norm(x0); LN_WAIT(18, x0);
  const u16* a = p.Wo + (size_t)(b & 7) * 512 * D_ + (((b >> 3) - 14) * 8 + w) * 2048 + lane * 8;
  while (rt() - t0 < ((unsigned)p.t1 >> 16 & 0xff) * 4) __builtin_amdgcn_s_sleep(4);
#pragma unroll
  for (int k = 0; k < 4; ++k) gla(pf[k], a + k * 512);
  const int G = lane >> 4, q4 = (lane >> 2) & 3, p4 = lane & 3, un = 2 * (w & 3) + (G & 1);
  const lptr ta = (8 * (G >> 1) + q4) * 512 + (p4 & 1) * 16 + (p4 >> 1) * 8, tb = ta + (8 | (un ^ (2 * q4) ^ 1)) * 32, tc = ta + (un ^ (2 * q4)) * 32;
  SCHED0();
  asm volatile("s_waitcnt vmcnt(4)" ::: "memory");
  BARN();
#pragma unroll
  for (int s = 0; s < 18; ++s) { W[s] = cat4(ldstr(tc + s * 8192), ldstr(tc + s * 8192 + 2048)); W[18 + s] = cat4(ldstr(tb + s * 8192), ldstr(tb + s * 8192 + 2048)); }
  BAR();
#pragma unroll
  for (int k = 0; k < 9; ++k) ldsw128((w >> 2) * XSEG + k * 4096 + (lane >> 3) * 512 + ((w & 3) * 8 + (lane & 7)) * 16, x0[k]);
  BAR();
  const lptr xa = (w >> 2) * XSEG + lane * 16;
  f32x16 acc = {};
#pragma unroll
  for (int q = 0; q < 36; q += 6) {
    v4u xb[6];
#pragma unroll
    for (int s = 0; s < 6; ++s) xb[s] = ldsr128(xa + (q + s) * 1024);
#pragma unroll
    for (int s = 0; s < 6; ++s) acc = mf3216(W[q + s], xb[s], acc);
  }
  const int hi = lane >> 5, j = lane & 31;
  const unsigned oo = WS_Q + mat * MATB + h * HB + ((w & 3) * 4 + hi) * 512 + (mat == 2 ? j ^ (8 * hi) : j) * 16 + (w >> 2) * SEGB;
  v4u o0, o1;
#pragma unroll
  for (int k = 0; k < 4; ++k) { o0[k] = pk(acc[2 * k], acc[2 * k + 1]); o1[k] = pk(acc[8 + 2 * k], acc[9 + 2 * k]); }
  bs128(o0, rs, oo, 0); bs128(o1, rs, oo + 1024, 0);
  if (w == 0) hs_wait(rs, b);
  asm volatile("s_waitcnt vmcnt(0)" :: "v"(pf[0]), "v"(pf[1]), "v"(pf[2]), "v"(pf[3]) : "memory");
  BAR();
  if (w == 0 && lane == 0) bs32(1, rs, 0, F_Q0 + (h * 4 + mat) * FS);
}

#define EG 3
#define EL 3
template <bool UP>
DEVI void e1_wg(const Params& p, const int i, const int w, const unsigned t00) {
  const int lane = LANE_ID("e1");
  const rsrc_t rs = mkrs(p.ws, WS_TOTAL);
  if (UP && T2(8)) return;
  if (UP) { while (rt() - t00 < (unsigned)((p.t2 >> 8) & 0xfff)) __builtin_amdgcn_s_sleep(8); }
  const int mat = i >> 4, hd = 4 * (i & 7) + 2 * ((i >> 3) & 1);
  const u16 *wq = p.Wq, *wk = p.Wk, *wv = p.Wv; asm volatile("" : "+s"(wq), "+s"(wk), "+s"(wv));
  const u16* sa = UP ? p.Wg + i * 128 : (mat == 0 ? wq : mat == 1 ? wk : wv) + hd * 128;
  const u16* sb = UP ? p.Wu + i * 128 : sa + 128;
  constexpr int LD = UP ? DF_ : HD_;
  constexpr int B = UP ? 0 : 2;
  if (!UP) { while (rt() - t00 < (unsigned)(p.t2 & 0xff) * 4) __builtin_amdgcn_s_sleep(8); }
  v4u W[36], x0[9];
  ln_load(p, B * 32 + w * 8, lane, x0);
  {
    const int up = (lane & 31) >> 1, r1 = lane >> 5;
    const int u = (up & 7) ^ (2 * (2 * (w & 1) + r1)) ^ (up >> 3);
    const u16* src = ((up >> 3) ? sb : sa) + (size_t)(2 * w + r1) * LD + u * 16 + (lane & 1) * 8;
#pragma unroll
    for (int k = 0; k < 18; ++k) dma16(src + (size_t)k * 16 * LD, (k * 8 + w) * 1024);
    v4u raw[18];
    LN_WAIT(18, x0); ln_norm(x0); LN_WAIT(18, x0);
#pragma unroll
    for (int k = 0; k < 18; ++k) raw[k] = gl128<UP ? 8 : 4>(src + (size_t)(18 + k) * 16 * LD);
    const int G = lane >> 4, q4 = (lane >> 2) & 3, p4 = lane & 3, a1 = G & 1;
    const int un = UP ? w + 8 * a1 : 8 * (w >> 2) + 2 * (w & 3) + a1;
    const lptr ta = (8 * (G >> 1) + q4) * 512 + ((un & 8) | ((un & 7) ^ (2 * q4) ^ (un >> 3))) * 32 + (p4 & 1) * 16 + (p4 >> 1) * 8;
    SCHED0();
    asm volatile("s_waitcnt vmcnt(18)" ::: "memory");
    BARN();
#pragma unroll
    for (int s = 0; s < 18; ++s) W[s] = cat4(ldstr(ta + s * 8192), ldstr(ta + s * 8192 + 2048));
    BAR();
#pragma unroll
    for (int k = 0; k < 18; ++k) ldsw128((k * 8 + w) * 1024 + lane * 16, raw[k]);
    BAR();
#pragma unroll
    for (int s = 0; s < 18; ++s) W[18 + s] = cat4(ldstr(ta + s * 8192), ldstr(ta + s * 8192 + 2048));
  }
  unsigned budget = 1u << 16;
  v2u gr = {};
  int fl = 0;
  if (w == 0) gr = bl64(rs, 0, H_G + (i + (UP ? 120 : 72)) * 8);
  BAR();
#pragma unroll
  for (int k = 0; k < 9; ++k) ldsw128((w >> 2) * XSEG + k * 4096 + (lane >> 3) * 512 + ((w & 3) * 8 + (lane & 7)) * 16, x0[k]);
  const unsigned l16 = lane * 16;
  const unsigned p5 = (w < 4 ? 32 + w : w) * 1024;
  v4u xr[5];
  auto xload = [&](int t) {
    const unsigned so = WS_XN + t * XSEG;
#pragma unroll
    for (int k = 0; k < 4; ++k) xr[k] = bl128(rs, l16 + (k * 8 + w) * 1024, so);
    xr[4] = bl128(rs, l16 + p5, so);
  };
  auto xput = [&](int t) {
    const lptr b = (t & 1) * XSEG + l16;
#pragma unroll
    for (int k = 0; k < 4; ++k) ldsw128(b + (k * 8 + w) * 1024, xr[k]);
    ldsw128(b + p5, xr[4]);
  };
  const int hi = lane >> 5, j = lane & 31;
  const unsigned oo = UP ? WS_H + (i * 16 + 2 * w + hi) * 512 + j * 16
                         : WS_Q + mat * MATB + (hd + (w >> 2)) * HB + ((w & 3) * 4 + hi) * 512 + (mat == 2 ? j ^ (8 * hi) : j) * 16;
  const unsigned fo = UP ? F_H + i * FS : F_QKV + ((i & 15) * 4 + mat) * FS;
  for (int t = B; t < 64; ++t) {
    BAR();
    if (t == B + 1) xload(B + 2);
    if (w == 0 && lane == 0 && t > B) bs32(t < B + 3 ? t - 1 : t - 2, rs, 0, fo);
    const lptr b = (t & 1) * XSEG + l16;
    f32x16 acc = {};
    v4u xb[2][EG];
#pragma unroll
    for (int s = 0; s < EG; ++s) xb[0][s] = ldsr128(b + s * 1024);
#pragma unroll
    for (int q = 0; q < 36 / EG; ++q) {
      if (q == 36 / EG - EL) {
        VM0();
        if (t == B && w == 0) { hs_wait(rs, i + (UP ? 120 : 72), gr); fl = (int)bl32(rs, (lane & 31) * FS, F_LN); }
        if (t > B && t < 63) xput(t + 1);
        if (t > B && t < 62) xload(t + 2);
      }
      if (q < 36 / EG - 1) {
#pragma unroll
        for (int s = 0; s < EG; ++s) xb[(q + 1) & 1][s] = ldsr128(b + ((q + 1) * EG + s) * 1024);
      }
      SCHED0();
#pragma unroll
      for (int s = 0; s < EG; ++s) acc = mf3216(W[q * EG + s], xb[q & 1][s], acc);
      SCHED0();
    }
    if (UP) {
      v4u o;
#pragma unroll
      for (int k = 0; k < 4; ++k) o[k] = pk(gelu_mul(acc[2 * k], acc[8 + 2 * k]), gelu_mul(acc[2 * k + 1], acc[9 + 2 * k]));
      if (!T2(1)) bs128(o, rs, oo, t * HSEG); else asm volatile("" :: "v"(o));
    } else {
      v4u o0, o1;
#pragma unroll
      for (int k = 0; k < 4; ++k) { o0[k] = pk(acc[2 * k], acc[2 * k + 1]); o1[k] = pk(acc[8 + 2 * k], acc[9 + 2 * k]); }
      if (!T2(1)) { bs128(o0, rs, oo, t * SEGB); bs128(o1, rs, oo + 1024, t * SEGB); } else asm volatile("" :: "v"(o0), "v"(o1));
    }
    if (t == B && w == 0 && !__all(fl >= 0)) wait_words(rs, F_LN, 32, 0, lane, budget);
    if (t < B + 2) VM0();
  }
  VM0();
  BAR();
  if (w == 0 && lane == 0) bs32(63, rs, 0, fo);
}

DEVI void e2_wg(const Params& p, const int e, const int w, const unsigned t00) {
  const int lane = LANE_ID("e2");
  const rsrc_t rs = mkrs(p.ws, WS_TOTAL);
  if (T2(4)) return;
  while (rt() - t00 < (unsigned)(p.t2 >> 20)) __builtin_amdgcn_s_sleep(8);
  const int ks = e & 7, cs = e >> 3;
  v4u W[9][2];
  {
    const lptr st = w * 18432;
    const u16* sd = p.Wd + (size_t)(576 * ks + 16 * w + (lane >> 3)) * D_ + 64 * cs + (lane & 7) * 8;
    const u16* so = p.Wo + (size_t)(512 * ks + 64 * w + (lane >> 3)) * D_ + 64 * cs + (lane & 7) * 8;
#pragma unroll
    for (int k = 0; k < 10; ++k) __builtin_amdgcn_global_load_lds((const GAS unsigned*)(sd + (size_t)((k >> 1) * (k >= 8 && w >= 4 ? 0 : 128) + (k & 1) * 8) * D_), LP(unsigned, st + k * 1024), 16, 0, AUX(8));
#pragma unroll
    for (int k = 0; k < 8; ++k) __builtin_amdgcn_global_load_lds((const GAS unsigned*)(so + (size_t)k * 8 * D_), LP(unsigned, st + 10240 + k * 1024), 16, 0, AUX(8));
    const v4u ones = {~0u, ~0u, ~0u, ~0u};
#pragma unroll
    for (int k = 0; k < 4; ++k) bs128(ones, rs, lane * 16, WS_EP + ((cs * 8 + w) * 8 + ks) * 1024 + k * EP_SLOT);
    VM0();
    BARN();
    if (w == 0 && lane == 0) bs32(0, rs, 0, F_E + e * FS);
    const int G = lane >> 4, q4 = (lane >> 2) & 3, p4 = lane & 3;
    const lptr ta = st + (8 * (G >> 1) + q4) * 128 + (G & 1) * 32 + p4 * 8;
#pragma unroll
    for (int s = 0; s < 9; ++s)
#pragma unroll
      for (int c = 0; c < 2; ++c) W[s][c] = cat4(ldstr(ta + s * 2048 + c * 64), ldstr(ta + s * 2048 + c * 64 + 512));
  }
  const unsigned l16 = lane * 16;
  const int hi = lane >> 5, j = lane & 31;
  const unsigned ho = WS_H + (36 * ks + w) * 1024, oo = WS_O + (4 * ks + (w >> 1)) * HB + (w & 1) * 4096;
  constexpr unsigned XBUF = 8u * 8448u, L_CTL = 2u * XBUF;
  const lptr xw = w * 8448 + hi * 528 + j * 16, xr = (lane & 15) * 528 + (4 * w + (lane >> 4)) * 16;
  const unsigned pw = WS_EP + ((cs * 8 + w) * 8 + ks) * 1024, pr = WS_EP + (cs * 8 + ks) * 8192, gv = (lane >> 3) * 1024 + (w * 8 + (lane & 7)) * 16;
  const unsigned xo = ((4 * ks + (w >> 1)) * D_ + 64 * cs + (w & 1) * 32 + (lane & 7) * 4) * 2;
  const unsigned long wsa = (unsigned long)p.ws, xa = (unsigned long)p.x;
  const v4u rx = {sfl((unsigned)xa), sfl((unsigned)(xa >> 32) & 0xffffu), S_ * D_ * 2, 0x00020000u};
  unsigned budget = 1u << 18;
  auto sel = [&](bool on) { const v4u r = {sfl((unsigned)wsa), sfl((unsigned)(wsa >> 32) & 0xffffu), sfl(on ? WS_TOTAL : 0u), 0x00020000u}; return r; };
#define ALD(n, dst, r, vo, so, mod) asm volatile("buffer_load_dword" n " %0, %1, %2, %3 offen " mod : "+v"(dst) : "v"(vo), "s"(r), "s"(so) : "memory")
#define AWT(n, x) asm volatile("s_waitcnt vmcnt(%1)" : "+v"(x) : "n"(n) : "memory")
  const unsigned fo = lane < 8 ? F_O + (ks * 8 + lane) * FS : F_H + (((9 * ks) >> 1) + min(lane - 8, 4)) * FS;
  auto ready = [&](int v, int to, int th) { const unsigned long long m = __ballot(v >= (lane < 8 ? to : th)); return (unsigned)((m & 0xff) == 0xff) | (unsigned)((m | 0xff) == ~0ull) << 1; };
  auto block = [&](int to, int th) {
    if (w == 7) {
      for (;;) {
        asm volatile("" ::: "memory");
        const int v = (int)bl32(rs, fo, 0);
        if (ready(v, to, th) == 3 || budget == 0) break;
        --budget;
        __builtin_amdgcn_s_sleep(2);
      }
    }
    BARN();
  };
  v4u f[5] = {}, d[4] = {}, g = {};
  v2u xv = {};
  int pv = 0;
  const unsigned hv1 = l16 + 8192, hv2 = l16 + 16384, hv3 = l16 + 24576, hv4 = w < 4 ? l16 + 32768 : 0x7fffff00u;
  auto hload = [&](v4u r, int t) {
    const unsigned so = sfl(ho + t * HSEG);
    ALD("x4", f[0], r, l16, so, "sc1"); ALD("x4", f[1], r, hv1, so, "sc1"); ALD("x4", f[2], r, hv2, so, "sc1"); ALD("x4", f[3], r, hv3, so, "sc1"); ALD("x4", f[4], r, hv4, so, "sc1");
  };
  auto dload = [&](v4u r, int t) {
    const unsigned so = sfl(oo + t * SEGB);
    ALD("x4", d[0], r, l16, so, "sc1"); ALD("x4", d[1], r, l16, so, "offset:1024 sc1"); ALD("x4", d[2], r, l16, so, "offset:2048 sc1"); ALD("x4", d[3], r, l16, so, "offset:3072 sc1");
  };
  auto finish = [&](int t, v4u gg, v2u xx) {
    const unsigned tag = (t >> 2) & 1;
    while (__any(((gg.x ^ tag) | (gg.z ^ tag)) & 1) && budget != 0) {
      --budget;
      const v4u r = sel(true); const unsigned so = sfl(pr + (t & 3) * EP_SLOT);
      ALD("x4", gg, r, gv, so, "sc1"); AWT(0, gg);
    }
    float r[4];
#pragma unroll
    for (int k = 0; k < 4; ++k) {
      float a = uf(gg[k]); a += ror8(a);
      auto s1 = __builtin_amdgcn_permlane16_swap(fu(a), fu(a), false, false); a = uf(s1[0]) + uf(s1[1]);
      auto s2 = __builtin_amdgcn_permlane32_swap(fu(a), fu(a), false, false); r[k] = uf(s2[0]) + uf(s2[1]);
    }
    const v2u ov = {pk(r[0] + bflo(xx.x), r[1] + bfhi(xx.x)), pk(r[2] + bflo(xx.y), r[3] + bfhi(xx.y))};
    if (lane < 8) *(GAS v2u*)((GAS char*)p.out + xo + (size_t)t * 32 * D_ * 2) = ov;
  };
#define EST(k)
  asm volatile("s_waitcnt lgkmcnt(0)" ::: "memory");
  if (w == 7) wait_words(rs, F_E + cs * 8 * FS, 8, 0, lane, budget);
  block(-1, 0);
  hload(sel(true), 0);
  VM0();
  const v4u rp = sel(w == 7);
  unsigned rdy = 0;
  for (int t = 0; t < 64; ++t) {
    f32x16 a0 = {}, a1 = {};
    EST(0);
#pragma unroll
    for (int k = 0; k < 4; ++k) { AWT(9 - k, f[k]); a0 = mf3216(W[k][0], f[k], a0); a1 = mf3216(W[k][1], f[k], a1); SCHED0(); }
    if (w < 4) { AWT(5, f[4]); a0 = mf3216(W[4][0], f[4], a0); a1 = mf3216(W[4][1], f[4], a1); }
    SCHED0();
    if (rdy != 3) {
      block(t, min(t + 1, 63));
      if (!(rdy & 1)) dload(sel(true), t);
    }
    EST(1);
    SCHED0();
    { const v4u r = sel(t > 1); const unsigned so = sfl(pr + ((t - 2) & 3) * EP_SLOT), sx = sfl(max(t - 2, 0) * 32 * D_ * 2), z = 0;
      ALD("x4", g, r, gv, so, "sc1"); ALD("x2", xv, rx, xo, sx, ""); ALD("", pv, rp, fo, z, "sc1"); }
    hload(sel(t < 63), t + 1);
    SCHED0();
#pragma unroll
    for (int k = 0; k < 4; ++k) { AWT(11 - k, d[k]); a0 = mf3216(W[5 + k][0], d[k], a0); a1 = mf3216(W[5 + k][1], d[k], a1); SCHED0(); }
    EST(2);
    const lptr xb = (t & 1) * XBUF;
#pragma unroll
    for (int q = 0; q < 4; ++q) {
      const v4u u0 = {fu(a0[4 * q]), fu(a0[4 * q + 1]), fu(a0[4 * q + 2]), fu(a0[4 * q + 3])}, u1 = {fu(a1[4 * q]), fu(a1[4 * q + 1]), fu(a1[4 * q + 2]), fu(a1[4 * q + 3])};
      ldsw128(xb + xw + q * 1056, u0); ldsw128(xb + xw + (4 + q) * 1056, u1);
    }
    EST(3);
    asm volatile("s_waitcnt vmcnt(5)" : "+v"(g), "+v"(xv), "+v"(pv) :: "memory");
    if (w == 7) ldsw32(L_CTL + (t & 1) * 4, t < 63 ? ready(pv, t + 1, min(t + 2, 63)) : 0u);
    if (t > 1) finish(t - 2, g, xv);
    EST(4);
    BAR();
    EST(5);
    const unsigned c = *LP(const unsigned, L_CTL + (t & 1) * 4);
    f32x4 sm = __builtin_bit_cast(f32x4, ldsr128(xb + xr));
#pragma unroll
    for (int v = 1; v < 8; ++v) sm += __builtin_bit_cast(f32x4, ldsr128(xb + xr + v * 8448));
    const unsigned tag = (t >> 2) & 1;
    v4u o = __builtin_bit_cast(v4u, sm); o.x = (o.x & ~1u) | tag; o.z = (o.z & ~1u) | tag;
    bs128(o, rs, l16, pw + (t & 3) * EP_SLOT);
    rdy = sfl(c);
    SCHED0();
    dload(sel(rdy & 1), t + 1);
    EST(6);
  }
  {
    const rsrc_t rxx = mkrs(p.x, S_ * D_ * 2);
    const v4u g0 = bl128(rs, gv, pr + 2 * EP_SLOT), g1 = bl128(rs, gv, pr + 3 * EP_SLOT);
    const v2u x0 = __builtin_amdgcn_raw_buffer_load_b64(rxx, xo, 62 * 32 * D_ * 2, 0), x1 = __builtin_amdgcn_raw_buffer_load_b64(rxx, xo, 63 * 32 * D_ * 2, 0);
    finish(62, g0, x0); finish(63, g1, x1);
  }
}

constexpr int DEF_T0 = 120, DEF_T1 = 32, DEF_GAP = 57, DEF_UP = 6, DEF_DN = 16, DEF_FF = 16, DEF_DU = 900, DEF_DE = 900, DEF_A0 = 24, DEF_AM = 40, DEF_S0 = 4, DEF_XF = 20, DEF_PD = 300, DEF_LD = 200, DEF_QD = 0;
__global__ void __launch_bounds__(512, 2) fused_k(Params p_) {
  const int b0 = blockIdx.x, w0 = __builtin_amdgcn_readfirstlane(threadIdx.x >> 6);
  hs_start(p_, b0, w0);
  if (b0 < 72) { chain_wg(b0, w0); return; }
  ROLE_ENTER; (void)rs; (void)dg; (void)w3;
  const unsigned t00 = rt();
  if (b >= 120 && b < 216) e0_wg(p, b, w);
  if (b >= 216 && b != 228) ln_seg(p, b, w, t00);
  if (b < 120) e1_wg<false>(p, b - 72, w, t00);
  else if (b < 156) e1_wg<true>(p, b - 120, w, t00);
  else if (b < 228) e2_wg(p, b - 156, w, t00);
  else if (b == 228) { pf_all(p, b, w, t00); if (w == 0) clock_wg(p); }
}
extern "C" void kernel_launch(void* const* d_in, const int* in_sizes, int n_in, void* d_out, int out_size, void* d_ws, size_t ws_size, hipStream_t stream) {
  Params p; p.x = (const u16*)d_in[0]; p.mem0 = (const u16*)d_in[1]; p.Wq = (const u16*)d_in[2]; p.Wk = (const u16*)d_in[3]; p.Wv = (const u16*)d_in[4]; p.Wo = (const u16*)d_in[5];
  p.Wg = (const u16*)d_in[8]; p.Wu = (const u16*)d_in[9]; p.Wd = (const u16*)d_in[10]; p.out = (u16*)d_out; p.ws = (unsigned char*)(((size_t)d_ws + (2u << 20) - 1) & ~(size_t)((2u << 20) - 1));
  p.t0 = DEF_T0 | DEF_A0 << 8 | DEF_AM << 16 | DEF_S0 << 24; p.t1 = DEF_T1 | DEF_XF << 8 | DEF_PD / 4 << 16 | DEF_LD / 4 << 24; p.t2 = DEF_QD / 4 | DEF_DU << 8 | DEF_DE << 20; p.t3 = DEF_GAP | DEF_UP << 8 | DEF_DN << 16 | DEF_FF << 24;
  (void)hipFuncSetAttribute((const void*)fused_k, hipFuncAttributeMaxDynamicSharedMemorySize, L_END);
  fused_k<<<NWG, 512, L_END, stream>>>(p);
}
```
